```python
import torch

def _softmax(x, axis=-1):
    return torch.softmax(x, dim=axis)
def _gelu(x):
    return torch.nn.functional.gelu(x, approximate="tanh")
def _silu(x):
    return torch.nn.functional.silu(x)
def _sigmoid(x):
    return torch.sigmoid(x)
def _layernorm(x, axis=-1, eps=1e-5):
    m = x.mean(dim=axis, keepdim=True)
    v = x.var(dim=axis, keepdim=True, unbiased=False)
    return (x - m) / torch.sqrt(v + eps)
def _scatter_add(tgt, idx, src, axis=0):
    return tgt.clone().index_add_(axis, idx, src)
def _segment_sum(src, seg, n):
    out = torch.zeros((n,) + src.shape[1:], dtype=src.dtype)
    return out.index_add_(0, seg, src)
def _pad(x, pw):
    flat = tuple(v for pair in reversed(pw) for v in pair)
    return torch.nn.functional.pad(x, flat)

kernel_name = 'recurrent_scan__ssm_conv_s4__mixer_token_mlp__prompt'

def setup_inputs():

    _w = (lambda a: (lambda t: t.to(torch.bfloat16) if t.is_floating_point() else t)(torch.as_tensor(a)))
    def _A_setup():
        B, L, D, N = 1, 2048, 512, 128
        A_re = -(lambda _x: torch.exp(torch.as_tensor(_x)))((lambda *s: torch.randn(*s, dtype=torch.bfloat16) * 3.2)(D, N))
        A_im = torch.pi * (lambda *s: torch.randn(*s, dtype=torch.bfloat16) * 0.2)(D, N)
        B_re = (lambda *s: torch.randn(*s, dtype=torch.bfloat16) * 0.2)(D, N)
        B_im = (lambda *s: torch.randn(*s, dtype=torch.bfloat16) * 0.2)(D, N)
        C_re = (lambda *s: torch.randn(*s, dtype=torch.bfloat16) * 0.2)(D, N)
        C_im = (lambda *s: torch.randn(*s, dtype=torch.bfloat16) * 0.2)(D, N)
        log_dt = (lambda *s: torch.randn(*s, dtype=torch.bfloat16) * 0.2)(D) * 0.5 - 4.0
        Dskip = (lambda *s: torch.randn(*s, dtype=torch.bfloat16) * 0.2)(D)
        return {
            "u": _w((lambda *s: torch.randn(*s, dtype=torch.bfloat16) * 0.05)(B, L, D)),
            "A_re": _w(A_re),
            "A_im": _w(A_im),
            "B_re": _w(B_re),
            "B_im": _w(B_im),
            "C_re": _w(C_re),
            "C_im": _w(C_im),
            "log_dt": _w(log_dt),
            "Dskip": _w(Dskip),
        }
    _A_in = _A_setup()

    def _B_setup():
        B, S, D, F = 1, 2048, 512, 1792
        return {
            "x": _w((lambda *s: torch.randn(*s, dtype=torch.bfloat16) * 0.05)(B, S, D)),
            "w1": _w((lambda *s: torch.randn(*s, dtype=torch.bfloat16) * 0.8)(S, F) * (S ** -0.5)),
            "b1": _w((lambda *s: torch.randn(*s, dtype=torch.bfloat16) * 12.8)(F) * 0.02),
            "w2": _w((lambda *s: torch.randn(*s, dtype=torch.bfloat16) * 0.2)(F, S) * (F ** -0.5)),
            "b2": _w((lambda *s: torch.randn(*s, dtype=torch.bfloat16) * 1.6)(S) * 0.02),
        }
    _B_in = _B_setup()

    return {
        "A_u": _A_in["u"],
        "A_A_re": _A_in["A_re"],
        "A_A_im": _A_in["A_im"],
        "A_B_re": _A_in["B_re"],
        "A_B_im": _A_in["B_im"],
        "A_C_re": _A_in["C_re"],
        "A_C_im": _A_in["C_im"],
        "A_log_dt": _A_in["log_dt"],
        "A_Dskip": _A_in["Dskip"],
        "B_x": _B_in["x"],
        "B_w1": _B_in["w1"],
        "B_b1": _B_in["b1"],
        "B_w2": _B_in["w2"],
        "B_b2": _B_in["b2"],
    }

def reference(A_u, A_A_re, A_A_im, A_B_re, A_B_im, A_C_re, A_C_im, A_log_dt, A_Dskip, B_x, B_w1, B_b1, B_w2, B_b2):
    def _A_ref(u, A_re, A_im, B_re, B_im, C_re, C_im, log_dt, Dskip):
        Bsz, L, D = u.shape
        dt = torch.exp(log_dt)[:, None]
        A = A_re + 1j * A_im
        Bc = B_re + 1j * B_im
        Cc = C_re + 1j * C_im
        logdA = dt * A
        dB = dt * Bc
        l = (lambda x, dt: x.to(dt))(torch.arange(L), A_re.dtype)
        P = torch.exp(logdA[:, :, None] * l[None, None, :])
        K = torch.einsum("dn,dnl,dn->dl", Cc, P, dB).real
        L2 = 2 * L
        Kp = _pad(K, ((0, 0), (0, L)))
        up = (lambda x, *d: x.permute(*d))(u, 0, 2, 1)
        up = _pad(up, ((0, 0), (0, 0), (0, L)))
        Kf = (lambda x, axes=(-2,-1): torch.fft.rfft2(x.float(), dim=axes))(Kp[:, None, :], axes=(-2, -1))
        Uf = (lambda x, axes=(-2,-1): torch.fft.rfft2(x.float(), dim=axes))(up[:, :, None, :], axes=(-2, -1))
        Yf = Uf * Kf[None, :, :, :]
        y = (lambda x, s, axes=(-2,-1): torch.fft.irfft2(x, s=s, dim=axes).to(torch.bfloat16))(Yf, (1, L2), axes=(-2, -1))[:, :, 0, :L]
        y = (lambda x, *d: x.permute(*d))(y, 0, 2, 1) + u * Dskip[None, None, :]
        return y

    def _B_ref(x, w1, b1, w2, b2):
        B, S, D = x.shape
        h = _layernorm(x, axis=-1)
        u = torch.einsum("bsd,sf->bdf", h, w1) + b1
        u = _gelu(u)
        y = torch.einsum("bdf,fs->bds", u, w2) + b2
        return (lambda x, *d: x.permute(*d))(y, 0, 2, 1)

    x = A_u
    xn = _layernorm(x, axis=-1)
    s = _A_ref(xn, A_A_re, A_A_im, A_B_re, A_B_im, A_C_re, A_C_im, A_log_dt, A_Dskip)
    g = _silu(_B_ref(xn, B_w1, B_b1, B_w2, B_b2))
    return x + s * g
```

```cpp
#include <hip/hip_runtime.h>
#include <hip/hip_bf16.h>
#include <stdint.h>

typedef short s16x4 __attribute__((ext_vector_type(4)));
typedef short bf16x8 __attribute__((ext_vector_type(8)));
typedef _Float16 f16x8 __attribute__((ext_vector_type(8)));
typedef _Float16 h2_t __attribute__((ext_vector_type(2)));
typedef __bf16 bf2_t __attribute__((ext_vector_type(2)));
typedef float f32x2 __attribute__((ext_vector_type(2)));
typedef float f32x4 __attribute__((ext_vector_type(4)));
typedef unsigned u32x2 __attribute__((ext_vector_type(2)));
typedef unsigned u32x4 __attribute__((ext_vector_type(4)));
typedef const unsigned short* cus;
#define DI __device__ __forceinline__
#define LDSP __attribute__((address_space(3)))

#define STW(k, slot, wv)
#define STDECL
#define STDUMP(wv)
#define STARG
#define STPASS
#define RW1 5
#define RW2 5
#define WL 7
#define AD 3
#define PBK 6
#define CGATE 2
#define IDEL 550
#define SDEL 32
#define SBK 16
#define HSP 1
#define GSP 32
#define HDEL 8
#define PSTEP 12
#define XPR 3
#define PFA 3
#define SPF 0
#define APOL " sc1"
constexpr int L_ = 2048, D_ = 512, N_ = 128, F_ = 1792;
constexpr int TABN = 648;
constexpr float LOG2E = 1.44269504088896341f;
constexpr int SPIN_CAP = 1 << 18;

DI unsigned pk_bf16(float lo, float hi) {
  f32x2 f = {lo, hi};
  return __builtin_bit_cast(unsigned, __builtin_convertvector(f, bf2_t));
}
DI unsigned pk_f16(float lo, float hi) {
  f32x2 f = {lo, hi};
  return __builtin_bit_cast(unsigned, __builtin_convertvector(f, h2_t));
}
DI float bf_lo(unsigned u) { return __uint_as_float(u << 16); }
DI float bf_hi(unsigned u) { return __uint_as_float(u & 0xffff0000u); }
DI float rbf(float x) { return bf_lo(pk_bf16(x, 0.f)); }
DI float bf2f(unsigned short u) { return __uint_as_float(((unsigned)u) << 16); }

template <int CTRL, int RM, bool BC> DI float dppf(float v) {
  return __builtin_bit_cast(float, __builtin_amdgcn_update_dpp(0, __builtin_bit_cast(int, v), CTRL, RM, 0xf, BC));
}
DI float wave_sum(float v) {
  v += dppf<0x111, 0xf, true>(v);
  v += dppf<0x112, 0xf, true>(v);
  v += dppf<0x114, 0xf, true>(v);
  v += dppf<0x118, 0xf, true>(v);
  v += dppf<0x142, 0xa, false>(v);
  v += dppf<0x143, 0xc, false>(v);
  return __builtin_bit_cast(float, __builtin_amdgcn_readlane(__builtin_bit_cast(int, v), 63));
}
DI float wave_max_nn(float v) {
  v = fmaxf(v, dppf<0x111, 0xf, true>(v));
  v = fmaxf(v, dppf<0x112, 0xf, true>(v));
  v = fmaxf(v, dppf<0x114, 0xf, true>(v));
  v = fmaxf(v, dppf<0x118, 0xf, true>(v));
  v = fmaxf(v, dppf<0x142, 0xa, false>(v));
  v = fmaxf(v, dppf<0x143, 0xc, false>(v));
  return __builtin_bit_cast(float, __builtin_amdgcn_readlane(__builtin_bit_cast(int, v), 63));
}
DI void touch(unsigned ldum, const void* p) { asm volatile("s_mov_b32 m0, %0\n\ts_nop 0\n\tglobal_load_lds_dword %1, off" :: "s"(ldum), "v"(p) : "memory"); }
DI void lds_barrier() { asm volatile("s_waitcnt lgkmcnt(0)" ::: "memory"); __builtin_amdgcn_s_barrier(); asm volatile("" ::: "memory"); }
DI void st16_dev(void* p, u32x4 v) { asm volatile("global_store_dwordx4 %0, %1, off sc1\n\ts_nop 1" :: "v"(p), "v"(v) : "memory"); }
DI void st8_dev(void* p, u32x2 v) { asm volatile("global_store_dwordx2 %0, %1, off sc1" :: "v"(p), "v"(v) : "memory"); }
DI void st4_dev(void* p, unsigned v) { asm volatile("global_store_dword %0, %1, off sc1" :: "v"(p), "v"(v) : "memory"); }
DI unsigned ld4_dev_wait(const void* p) { unsigned r; asm volatile("global_load_dword %0, %1, off sc1\n\ts_waitcnt vmcnt(0)" : "=&v"(r) : "v"(p) : "memory"); return r; }

DI void grp_arrive(volatile LDSP unsigned* words, int idx, unsigned ph, int lane) {
  asm volatile("s_waitcnt lgkmcnt(0)" ::: "memory");
  if (lane == 0) words[idx] = ph;
}
template <int SL = 1>
DI void grp_wait(volatile LDSP unsigned* words, unsigned ph) {
#pragma unroll 1
  for (int sp = 0; sp < SPIN_CAP * 8; ++sp) {
    const u32x4 v = *(volatile LDSP u32x4*)words;
    const unsigned m = min(min(v[0], v[1]), min(v[2], v[3]));
    if (__builtin_amdgcn_readfirstlane(m) >= ph) break;
    if (SL) __builtin_amdgcn_s_sleep(SL);
  }
  asm volatile("" ::: "memory");
}
template <int SL = 1>
DI void grp_bar(volatile LDSP unsigned* words, int idx, unsigned ph, int lane) { grp_arrive(words, idx, ph, lane); grp_wait<SL>(words, ph); }
template <int SL = 1>
DI void word_wait(volatile LDSP unsigned* word, unsigned v) {
#pragma unroll 1
  for (int sp = 0; sp < SPIN_CAP * 8; ++sp) {
    if (__builtin_amdgcn_readfirstlane(*word) >= v) break;
    if (SL) __builtin_amdgcn_s_sleep(SL);
  }
  asm volatile("" ::: "memory");
}
template <int BK = PBK>
DI void poll_flags(const unsigned* p, unsigned tag, bool done) {
#pragma unroll 1
  for (int sp = 0; sp < SPIN_CAP; ++sp) {
    if (!done) done = ld4_dev_wait(p) == tag;
    if (__all(done)) break;
    __builtin_amdgcn_s_sleep(BK);
  }
}
DI void poll_flags4(const unsigned* p, unsigned tag) {
  bool done = false;
#pragma unroll 1
  for (int sp = 0; sp < SPIN_CAP; ++sp) {
    if (!done) {
      u32x4 f;
      asm volatile("global_load_dwordx4 %0, %1, off sc1\n\ts_waitcnt vmcnt(0)" : "=&v"(f) : "v"(p) : "memory");
      done = f[0] == tag && f[1] == tag && f[2] == tag && f[3] == tag;
    }
    if (__all(done)) break;
    __builtin_amdgcn_s_sleep(PBK);
  }
}

DI int kpos(int kb) { return kb ^ (((kb >> 2) & 1) << 1); }

template <bool PIVOT>
DI void ln8(float (&v)[8], u32x4& o) {
  const float p = PIVOT ? __builtin_bit_cast(float, __builtin_amdgcn_readfirstlane(__builtin_bit_cast(int, v[0]))) : 0.f;
  float s0 = 0.f, s1 = 0.f, q0 = 0.f, q1 = 0.f;
#pragma unroll
  for (int i = 0; i < 4; ++i) { const float t = v[i] - p, u = v[i + 4] - p; s0 += t; s1 += u; q0 = __builtin_fmaf(t, t, q0); q1 = __builtin_fmaf(u, u, q1); }
  const float ds = wave_sum(s0 + s1) * (1.f / D_), dq = wave_sum(q0 + q1) * (1.f / D_);
  const float mean = p + ds, var = fmaxf(__builtin_fmaf(-ds, ds, dq), 0.f);
  const float mb = rbf(mean), vb = rbf(var);
  const float den = rbf(__builtin_amdgcn_sqrtf(rbf(vb + 1e-5f)));
  const float inv = __builtin_amdgcn_rcpf(den);
#pragma unroll
  for (int i = 0; i < 4; ++i) {
    const unsigned t = pk_bf16(v[2 * i] - mb, v[2 * i + 1] - mb);
    o[i] = pk_bf16(bf_lo(t) * inv, bf_hi(t) * inv);
    v[2 * i] = bf_lo(o[i]); v[2 * i + 1] = bf_hi(o[i]);
  }
}
DI void cexp2(float le, float ph, float& re, float& im) {
  const float e = __builtin_amdgcn_exp2f(le);
  const float p = __builtin_amdgcn_fractf(ph);
  re = e * __builtin_amdgcn_cosf(p);
  im = e * __builtin_amdgcn_sinf(p);
}
DI float gelu_tanh(float x) {
  const float u = x * (1.f + 0.044715f * x * x);
  return x * __builtin_amdgcn_rcpf(1.f + __builtin_amdgcn_exp2f(-2.f * 0.7978845608028654f * LOG2E * u));
}
DI float silu(float x) { return x * __builtin_amdgcn_rcpf(1.f + __builtin_amdgcn_exp2f(-LOG2E * x)); }

#define DMA4(ldsaddr, vo, sb) do { unsigned keep_; \
    asm volatile("s_mov_b32 %0, m0\n\ts_mov_b32 m0, %1\n\ts_nop 0\n\t" \
                 "global_load_lds_dwordx4 %2, %6\n\tglobal_load_lds_dwordx4 %3, %6 offset:1024\n\t" \
                 "global_load_lds_dwordx4 %4, %6 offset:2048\n\tglobal_load_lds_dwordx4 %5, %6 offset:3072\n\ts_mov_b32 m0, %0" \
                 : "=&s"(keep_) : "s"(__builtin_amdgcn_readfirstlane(ldsaddr)), "v"(vo[0]), "v"(vo[1]), "v"(vo[2]), "v"(vo[3]), "s"(sb) : "memory"); } while (0)
template <int RB> DI void w_offsets(int lane, unsigned (&vo)[4]) {
  const int lr = lane >> 3, p8 = lane & 7;
#pragma unroll
  for (int j = 0; j < 4; ++j) vo[j] = (8 * j + lr) * RB + ((p8 ^ ((lr & 2) | ((j & 1) << 2))) << 4) - 1024 * j;
}
struct TrAddr { const char* p[4]; };
DI void tr_addr(TrAddr& t, const char* ring, int lane) {
  const int g = lane >> 4, q = (lane & 15) >> 2, p = lane & 3;
  const int frow = (q & 2) | ((g & 1) << 2);
  const char* trb = ring + (8 * g + q) * 128 + (p & 1) * 8;
#pragma unroll
  for (int j = 0; j < 4; ++j) t.p[j] = trb + (((2 * j + (p >> 1)) ^ frow) << 4);
}
DI void tr_read(bf16x8 (&fr)[4], const TrAddr& t, int off) {
#pragma unroll
  for (int j = 0; j < 4; ++j) {
    s16x4 lo = __builtin_amdgcn_ds_read_tr16_b64_v4i16((LDSP s16x4*)(t.p[j] + off));
    s16x4 hi = __builtin_amdgcn_ds_read_tr16_b64_v4i16((LDSP s16x4*)(t.p[j] + off + 512));
    fr[j] = __builtin_shufflevector(lo, hi, 0, 1, 2, 3, 4, 5, 6, 7);
  }
}
template <int RB, int OFF>
DI void w_issue(const char* wb, const unsigned (&vo)[4], unsigned lring, int k0, int k1) {
#pragma unroll 1
  for (int k = k0; k < k1; ++k) DMA4(lring + ((k + OFF) % WL) * 4096, vo, wb + (size_t)k * 128 * RB);
}
template <int RWN, int OFF>
DI void w_to_regs(const TrAddr& tra, bf16x8 (&wr)[RWN][4]) {
#pragma unroll
  for (int k = 0; k < RWN; ++k) tr_read(wr[k], tra, ((k + OFF) % WL) * 4096);
#pragma unroll
  for (int k = 0; k < RWN; ++k) asm volatile("s_waitcnt lgkmcnt(0)" : "+v"(wr[k][0]), "+v"(wr[k][1]), "+v"(wr[k][2]), "+v"(wr[k][3]) :: "memory");
}
template <int NKB, int RWN> struct Sch {
  static constexpr int NL = NKB - RWN;
  static constexpr int blk(int i) { return i < NL ? RWN + i : i - NL; }
  static constexpr int slot(int i) { return (RWN + i) % WL; }
  static constexpr bool dma(int s) { return s >= 1 && s - 1 + WL < NL; }
  static constexpr int younger(int i) {
    int pos = 0, pa = -1, pw = -1;
    for (int k = 0; k < AD; ++k) { if (k == i) pa = pos; ++pos; }
    for (int s = 0; s < i; ++s) {
      if (dma(s)) { if (s - 1 + WL == i + 1) pw = pos; ++pos; }
      if (s + AD < NKB) { if (s + AD == i) pa = pos; ++pos; }
    }
    const int need = pa > pw ? pa : pw;
    return pos - 1 - need;
  }
};
DI bf16x8 tr_frag(const char* p) {
  s16x4 lo = __builtin_amdgcn_ds_read_tr16_b64_v4i16((LDSP s16x4*)(p));
  s16x4 hi = __builtin_amdgcn_ds_read_tr16_b64_v4i16((LDSP s16x4*)(p + 512));
  return __builtin_shufflevector(lo, hi, 0, 1, 2, 3, 4, 5, 6, 7);
}
template <int NKB, int RB, bool SWAP, int RWN>
DI void gemm_main(const char* ab, const char* abo, const char* wb, const unsigned (&vo)[4], unsigned lring, const TrAddr& tra,
                  bf16x8 (&wr)[RWN][4], int lane, f32x4 (&acc)[4][4], volatile LDSP unsigned* sig, unsigned long long* lst = nullptr) {
  typedef Sch<NKB, RWN> S;
  const unsigned va = lane * 16;
  u32x4 av[AD][4];
  bf16x8 fr[4];
#define ABASE(i) (((S::blk(i) & 1) ? abo : ab) + (size_t)S::blk(i) * 16384)
#define LDA1(i, t) asm volatile("global_load_dwordx4 %0, %1, %2 offset:%3" APOL : "=&v"(av[(i) % AD][t]) : "v"(va), "s"(ABASE(i)), "i"((t) * 1024) : "memory")
#define DMA1(i, q) asm volatile("s_mov_b32 m0, %0\n\ts_nop 0\n\tglobal_load_lds_dwordx4 %1, %2 offset:%3" \
                 :: "s"(__builtin_amdgcn_readfirstlane(lring + S::slot((i) - 1) * 4096)), "v"(vo[q]), "s"(wb + (size_t)(RWN + (i) - 1 + WL) * 128 * RB), "i"((q) * 1024) : "memory")
#pragma unroll
  for (int i = 0; i < AD; ++i) { LDA1(i, 0); LDA1(i, 1); LDA1(i, 2); LDA1(i, 3); }
  asm volatile("s_waitcnt vmcnt(%0)" :: "i"(4 * AD) : "memory");
#pragma unroll
  for (int j = 0; j < 4; ++j) fr[j] = tr_frag(tra.p[j] + S::slot(0) * 4096);
#pragma unroll
  for (int i = 0; i < NKB; ++i) {
    const int st = i % AD;
    asm volatile("s_waitcnt vmcnt(%4)" : "+v"(av[st][0]), "+v"(av[st][1]), "+v"(av[st][2]), "+v"(av[st][3]) : "i"(4 * S::younger(i)) : "memory");
    if (!SWAP && i == PSTEP && lane == 0) *sig = 1;
#pragma unroll
    for (int j = 0; j < 4; ++j) {
#pragma unroll
      for (int t = 0; t < 4; ++t) {
        const bf16x8 wf = i < S::NL ? fr[j] : wr[i < S::NL ? 0 : i - S::NL][j];
        if (i == 0) {
          if (SWAP) asm volatile("v_mfma_f32_16x16x32_bf16 %0, %1, %2, 0" : "=&v"(acc[j][t]) : "v"(av[st][t]), "v"(wf) : "memory");
          else asm volatile("v_mfma_f32_16x16x32_bf16 %0, %1, %2, 0" : "=&v"(acc[j][t]) : "v"(wf), "v"(av[st][t]) : "memory");
        } else {
          if (SWAP) asm volatile("v_mfma_f32_16x16x32_bf16 %0, %1, %2, %0" : "+v"(acc[j][t]) : "v"(av[st][t]), "v"(wf) : "memory");
          else asm volatile("v_mfma_f32_16x16x32_bf16 %0, %1, %2, %0" : "+v"(acc[j][t]) : "v"(wf), "v"(av[st][t]) : "memory");
        }
        if (S::dma(i) && j < 2 && (t == 0 || t == 2)) { if (j == 0 && t == 0) DMA1(i, 0); else if (j == 0) DMA1(i, 1); else if (t == 0) DMA1(i, 2); else DMA1(i, 3); }
        if (j == 3 && i + AD < NKB) { if (t == 0) LDA1(i + AD, 0); else if (t == 1) LDA1(i + AD, 1); else if (t == 2) LDA1(i + AD, 2); else LDA1(i + AD, 3); }
      }
      if (i + 1 < S::NL) fr[j] = tr_frag(tra.p[j] + S::slot(i + 1) * 4096);
    }
  }
  asm volatile("s_nop 7\n\ts_nop 7" ::: "memory");
#undef LDA1
#undef DMA1
#undef ABASE
}
DI void reduce4(f32x4 (&acc)[4][4], f32x4 (&res)[4], char* lds, int w, int lane, volatile LDSP unsigned* words, unsigned ph) {
  static_assert(WL >= 3, "");
  char* mine = lds + w * (WL * 4096) + lane * 16;
#pragma unroll
  for (int t = 0; t < 4; ++t) {
    if (t != w) {
      const int slot = (t - w - 1) & 3;
#pragma unroll
      for (int j = 0; j < 4; ++j) *(f32x4*)(mine + slot * 4096 + j * 1024) = acc[j][t];
    } else {
#pragma unroll
      for (int j = 0; j < 4; ++j) res[j] = acc[j][t];
    }
  }
  grp_bar<SPF>(words, w, ph, lane);
#pragma unroll
  for (int k = 1; k < 4; ++k) {
    const int src = (w + k) & 3, slot = (w - src - 1) & 3;
    const char* p = lds + src * (WL * 4096) + slot * 4096 + lane * 16;
#pragma unroll
    for (int j = 0; j < 4; ++j) res[j] += *(const f32x4*)(p + j * 1024);
  }
}

struct Args {
  const u32x4* x; cus Are, Aim, Bre, Bim, Cre, Cim, logdt, Dskip, w1, b1, w2, b2;
  unsigned short* out;
  char* Hf;  char* X2; char* Gf; char* sT; unsigned* fl;
};
constexpr int FL_H = 64, FL_G = FL_H + 256 * 32, FL_S = FL_G + 256 * 32;

constexpr int KR = 2080;
constexpr int UPAD = 256;
constexpr int R1OFF = KR * 2 + 128;
constexpr int KT_B = R1OFF + KR * 2 + 32;
constexpr int U_B = (UPAD + L_) * 2;
constexpr int XR = 4 * WL * 4096;
constexpr int X_KT = XR, X_U = X_KT + 2 * KT_B, X_SC = X_U + 2 * U_B;
constexpr int SC_B = 8192 + 2 * TABN * 8;
constexpr int X_SY = X_SC + SC_B;
constexpr int X_B1 = X_SY + 128;
constexpr int X_DUM = X_B1 + 512;
constexpr int LDS_B = X_DUM + 256;
static_assert(LDS_B <= 160 * 1024 && SC_B >= 16384, "LDS");

__device__ __attribute__((noinline)) void code_end_marker();

__global__ __launch_bounds__(512, 2) void k_mega(Args a STARG) {
  extern __shared__ __attribute__((aligned(16))) char lds[];
  const int tid = threadIdx.x, lane = tid & 63, w = __builtin_amdgcn_readfirstlane(tid >> 6);
  const int b = blockIdx.x, xg = b & 7, r_ = b >> 3;
  const int tb = xg * 32 + r_;
  const unsigned lbase = (unsigned)(size_t)(LDSP char*)lds;
  volatile LDSP unsigned* sy = (volatile LDSP unsigned*)(lds + X_SY);
  STDECL
  STW(0, 0, 0);
  const bool has1 = r_ < 28;
  const int idx1 = xg * 28 + (has1 ? r_ : 0);
  const int fT = idx1 >> 3, dT = idx1 & 7;
  const int sTl = tb >> 3, dT2 = tb & 7;
  if (tid < 32) sy[tid] = 0;
  u32x4 xr; unsigned ep;
  unsigned praw[8] = {0, 0, 0, 0, 0, 0, 0, 0};
  const u32x4* xp = a.x + (size_t)(tb * 8 + w) * 64 + lane;
  if (w >= 4) {
    const int i = (tb * 2) * N_ + (w - 4) * 64 + lane;
    const int dch = tb * 2 + ((w - 4) >> 1);
    const int li = r_ * 256 + tid - 256, ft0 = (xg * 28) >> 3;
    const char* t1 = (const char*)a.w1 + (size_t)(li >> 2) * (F_ * 2) + (ft0 + (li & 3)) * 128;
    const char* t2 = t1;
    {
      unsigned long long pc;
      asm volatile("s_getpc_b64 %0" : "=s"(pc));
      const unsigned long long pe = (unsigned long long)(void*)&code_end_marker;
      const unsigned long long pa = (pc & ~127ull) + (unsigned long long)(((w - 4) * 4 + (r_ & 3)) * 64 + lane) * 128;
      if (pe > pc && pe - pc < 65536 && pa + 4 <= pe && r_ >= 28 && w < 6) t2 = (const char*)pa;
    }
    asm volatile("global_load_dwordx4 %0, %10, off\n\tglobal_load_dword %1, %11, off sc1\n\t"
                 "global_load_ushort %2, %12, off\n\tglobal_load_ushort %3, %13, off\n\tglobal_load_ushort %4, %14, off\n\tglobal_load_ushort %5, %15, off\n\t"
                 "global_load_ushort %6, %16, off\n\tglobal_load_ushort %7, %17, off\n\tglobal_load_ushort %8, %18, off\n\tglobal_load_ushort %9, %19, off\n\t"
                 "s_mov_b32 m0, %22\n\ts_barrier\n\tglobal_load_lds_dword %21, off\n\tglobal_load_lds_dword %20, off\n\ts_waitcnt vmcnt(2)"
                 : "=&v"(xr), "=&v"(ep), "=&v"(praw[0]), "=&v"(praw[1]), "=&v"(praw[2]), "=&v"(praw[3]), "=&v"(praw[4]), "=&v"(praw[5]), "=&v"(praw[6]), "=&v"(praw[7])
                 : "v"(xp), "v"(a.fl), "v"(a.Are + i), "v"(a.Aim + i), "v"(a.Bre + i), "v"(a.Bim + i), "v"(a.Cre + i), "v"(a.Cim + i),
                   "v"(a.logdt + dch), "v"(a.Dskip + dch), "v"(t1), "v"(t2), "s"(lbase + X_DUM) : "memory");
  } else {
    u32x4 bv;
    asm volatile("global_load_dwordx4 %0, %3, off\n\tglobal_load_dword %1, %4, off sc1\n\tglobal_load_dwordx4 %2, %5, off\n\ts_barrier\n\ts_waitcnt vmcnt(0)"
                 : "=&v"(xr), "=&v"(ep), "=&v"(bv) : "v"(xp), "v"(a.fl), "v"(a.b1 + fT * 64 + (lane & 7) * 8) : "memory");
    if (lane < 8) *(u32x4*)(lds + X_B1 + w * 128 + lane * 16) = bv;
  }
  const unsigned tag = __builtin_amdgcn_readfirstlane(ep) + 1u;
  STW(0, 1, 0);
  constexpr int RS = 1024 + 32;
  {
    float v[8];
#pragma unroll
    for (int j = 0; j < 4; ++j) { v[2 * j] = bf_lo(xr[j]); v[2 * j + 1] = bf_hi(xr[j]); }
    u32x4 o;
    ln8<true>(v, o);
    *(u32x4*)(lds + w * RS + lane * 16) = o;
    ln8<false>(v, o);
    *(u32x4*)(lds + (8 + w) * RS + lane * 16) = o;
  }
  lds_barrier();
  STW(0, 2, 0);

  if (w < 4) {
    __builtin_amdgcn_s_setprio(3);
    const int g = lane >> 4, c = lane & 15, q = (lane & 15) >> 2, p = lane & 3;
    {
      u32x4 ov[2][2];
#pragma unroll
      for (int arr = 1; arr >= 0; --arr)
#pragma unroll
        for (int it = 0; it < 2; ++it) {
          const char* base = lds + (arr * 8 + q) * RS + (w * 8 + it * 4 + g) * 32 + p * 8;
          s16x4 lo = __builtin_amdgcn_ds_read_tr16_b64_v4i16((LDSP s16x4*)(base));
          s16x4 hi = __builtin_amdgcn_ds_read_tr16_b64_v4i16((LDSP s16x4*)(base + 4 * RS));
          ov[arr][it] = __builtin_bit_cast(u32x4, __builtin_shufflevector(lo, hi, 0, 1, 2, 3, 4, 5, 6, 7));
        }
#pragma unroll
      for (int arr = 1; arr >= 0; --arr)
#pragma unroll
        for (int it = 0; it < 2; ++it) {
          const int dt = w * 8 + it * 4 + g;
          if (arr) st16_dev(a.Hf + ((size_t)((dt >> 2) * 64 + kpos(tb >> 2)) * 4 + (dt & 3)) * 1024 + ((tb & 3) * 16 + c) * 16, ov[arr][it]);
          else st16_dev(a.X2 + ((size_t)tb * 512 + dt * 16 + c) * 16, ov[arr][it]);
        }
    }
    const unsigned lring = lbase + w * (WL * 4096);
    TrAddr tra;
    tr_addr(tra, lds + w * (WL * 4096), lane);
    f32x4 acc[4][4], res[4];
    unsigned vo[4];
    w_offsets<F_ * 2>(lane, vo);
    const char* wb1 = (const char*)a.w1 + (size_t)(w * 32) * (F_ * 2) + fT * 128;
    const char* wb2 = (const char*)a.w2 + (size_t)(w * 32) * (L_ * 2) + sTl * 128;
    unsigned long long tH = 0;
    asm volatile("s_waitcnt vmcnt(0)" ::: "memory");
    grp_arrive(sy + 12, w, 1, lane);
    if (w == 0) {
      grp_wait<SPF>(sy + 12, 1);
      if (lane == 0) { st4_dev(a.fl + FL_H + tb * HSP, tag); sy[8] = 1; }
    } else word_wait<SPF>(sy + 8, 1);
    __builtin_amdgcn_s_setprio(0);
    STW(1, 0, 0);
    if (has1) {
      bf16x8 wr1[RW1][4];
      w_issue<F_ * 2, 0>(wb1, vo, lring, 0, WL);
      asm volatile("s_waitcnt vmcnt(%0)" :: "i"(4 * (WL - RW1)) : "memory");
      w_to_regs<RW1, 0>(tra, wr1);
      w_issue<F_ * 2, 0>(wb1, vo, lring, WL, WL + RW1);
      STW(1, 1, 0);
      if (w == 0) {
        poll_flags4(a.fl + FL_H + lane * 4 * HSP, tag);
        if (lane == 0) sy[11] = 1;
      } else word_wait<1>(sy + 11, 1);
      __builtin_amdgcn_s_setprio(3);
      STW(1, 2, 0);
      gemm_main<16, F_ * 2, false, RW1>(a.Hf + (size_t)(dT * 64 + w) * 4096, a.Hf + (size_t)(dT * 64 + (w ^ 2)) * 4096, wb1, vo, lring, tra, wr1, lane, acc, sy + 19
      );
      STW(1, 3, 0);
      reduce4(acc, res, lds, w, lane, sy, 1);
      STW(1, 4, 0);
      u32x2 o[4];
#pragma unroll
      for (int j = 0; j < 4; ++j) {
        const u32x2 bb = *(const u32x2*)(lds + X_B1 + w * 128 + (16 * j + 4 * g) * 2);
        const float v0 = gelu_tanh(res[j][0] + bf_lo(bb[0])), v1 = gelu_tanh(res[j][1] + bf_hi(bb[0]));
        const float v2 = gelu_tanh(res[j][2] + bf_lo(bb[1])), v3 = gelu_tanh(res[j][3] + bf_hi(bb[1]));
        o[j] = u32x2{pk_bf16(v0, v1), pk_bf16(v2, v3)};
      }
#pragma unroll
      for (int jp = 0; jp < 2; ++jp) {
        u32x4 q4;
#pragma unroll
        for (int e = 0; e < 2; ++e) {
          auto sw = __builtin_amdgcn_permlane16_swap(o[2 * jp][e], o[2 * jp + 1][e], false, false);
          q4[e] = sw[0]; q4[2 + e] = sw[1];
        }
        const int f = fT * 64 + 16 * (2 * jp + (g & 1)) + 8 * (g >> 1);
        st16_dev(a.Gf + ((size_t)(dT * 56 + kpos(f >> 5)) * 4 + w) * 1024 + (((f & 31) >> 3) * 16 + c) * 16, q4);
      }
      grp_arrive(sy + 24, w, 1, lane);
      w_offsets<L_ * 2>(lane, vo);
      asm volatile("s_waitcnt vmcnt(0)" ::: "memory");
      grp_arrive(sy + 12, w, 2, lane);
      if (w == 0) {
        grp_wait<SPF>(sy + 12, 2);
        if (lane == 0) { st4_dev(a.fl + FL_G + (dT * 32 + fT) * GSP, tag); sy[9] = 1; }
      } else word_wait<SPF>(sy + 9, 1);
      __builtin_amdgcn_s_setprio(0);
      STW(1, 5, 0);
    } else {
      const int bq = r_ - 28;
#pragma unroll 1
      for (int li = tid; li < 1792; li += 256) {
        if (li - tid == PFA * 256) {
          asm volatile("s_waitcnt vmcnt(0)" ::: "memory");
          if (w == 0) {
            __builtin_amdgcn_s_sleep(HDEL);
            poll_flags4(a.fl + FL_H + lane * 4 * HSP, tag);
            if (lane == 0) sy[11] = 1;
            tH = __builtin_amdgcn_s_memrealtime();
          }
          grp_wait<2>(sy + 4, 2);
        }
        touch(lbase + X_DUM, (const char*)a.w2 + (size_t)((li >> 2) * 4 + bq) * (L_ * 2) + (xg * 4 + (li & 3)) * 128);
        asm volatile("s_waitcnt vmcnt(3)" ::: "memory");
      }
      w_offsets<L_ * 2>(lane, vo);
      STW(1, 6, 0);
    }
    bf16x8 wr2[RW2][4];
    w_issue<L_ * 2, 0>(wb2, vo, lring, 0, WL);
    asm volatile("s_waitcnt vmcnt(%0)" :: "i"(4 * (WL - RW2)) : "memory");
    w_to_regs<RW2, 0>(tra, wr2);
    w_issue<L_ * 2, 0>(wb2, vo, lring, WL, WL + RW2);
    STW(2, 0, 0);
    if (w == 0) {
      if (!has1) {
#pragma unroll 1
        for (int sp = 0; sp < 4096 && __builtin_amdgcn_s_memrealtime() - tH < IDEL; ++sp) __builtin_amdgcn_s_sleep(4);
      }
      poll_flags<PBK>(a.fl + FL_G + (dT2 * 32 + (lane & 31)) * GSP, tag, lane >= 28);
      if (lane == 0) sy[11] = 2;
    } else word_wait<1>(sy + 11, 2);
    STW(2, 1, 0);
    __builtin_amdgcn_s_setprio(3);
    gemm_main<14, L_ * 2, true, RW2>(a.Gf + (size_t)(dT2 * 56 + w) * 4096, a.Gf + (size_t)(dT2 * 56 + (w ^ 2)) * 4096, wb2, vo, lring, tra, wr2, lane, acc, sy + 19
      );
    STW(2, 2, 0);
    u32x2 xv[4];
    unsigned braw[4];
    const size_t oo = (size_t)(sTl * 64 + c) * D_ + dT2 * 64 + 16 * w + 4 * g;
#pragma unroll
    for (int j = 0; j < 4; ++j) {
      braw[j] = a.b2[sTl * 64 + 16 * j + c];
      xv[j] = *(const u32x2*)((cus)a.x + oo + (size_t)(16 * j) * D_);
    }
    reduce4(acc, res, lds, w, lane, sy, 2);
    STW(2, 3, 0);
    word_wait<SPF>(sy + 10, 1);
    STW(2, 4, 0);
#pragma unroll
    for (int j = 0; j < 4; ++j) {
      const char* ap = lds + X_SC + (16 * w + 4 * g + q) * 144 + j * 32 + p * 8;
      const u32x2 sv = __builtin_bit_cast(u32x2, __builtin_amdgcn_ds_read_tr16_b64_v4i16((LDSP s16x4*)ap));
      const float bias = bf_lo(braw[j]);
      const float o0 = bf_lo(xv[j][0]) + bf_lo(sv[0]) * silu(res[j][0] + bias);
      const float o1 = bf_hi(xv[j][0]) + bf_hi(sv[0]) * silu(res[j][1] + bias);
      const float o2 = bf_lo(xv[j][1]) + bf_lo(sv[1]) * silu(res[j][2] + bias);
      const float o3 = bf_hi(xv[j][1]) + bf_hi(sv[1]) * silu(res[j][3] + bias);
      u32x2 o = {pk_bf16(o0, o1), pk_bf16(o2, o3)};
      *(u32x2*)(a.out + oo + (size_t)(16 * j) * D_) = o;
    }
    STW(2, 5, 0);
    STDUMP(0);
  } else {
    const int j4 = w - 4;
    volatile LDSP unsigned* xb = sy + 4;
    const int ch = j4 >> 1, hw = j4 & 1;
    const int d = tb * 2 + ch;
    {
      float* par = (float*)(lds + X_SC + j4 * 2048);
      float* tabf = (float*)(lds + X_SC + 8192 + ch * (TABN * 8));
      const float dt = rbf(__expf(bf_lo(praw[6])));
      const float zr = dt * bf_lo(praw[0]) * LOG2E, zi = dt * bf_lo(praw[1]) * 0.15915494309189535f;
      const float br = dt * bf_lo(praw[2]), bi = dt * bf_lo(praw[3]), cr = bf_lo(praw[4]), ci = bf_lo(praw[5]);
      float wr = cr * br - ci * bi, wi = cr * bi + ci * br;
      const float zmax = wave_max_nn(fmaxf(zr, 0.f));
      const float wmax = wave_max_nn(fmaxf(fabsf(wr), fabsf(wi)));
      const unsigned we = (__float_as_uint(wmax) >> 23) & 0xff;
      const unsigned wec = we < 16 ? 16 : (we > 250 ? 250 : we);
      const float wsc = __uint_as_float((266u - wec) << 23);
      const float osc = __uint_as_float((wec - 12u) << 23) * 6.103515625e-05f;
      const float zs = zr - zmax;
      wr *= wsc; wi *= wsc;
      float qr, qi, er, ei;
      cexp2(zs * 128.f, zi * 128.f, qr, qi);
      cexp2(zs * 1024.f, zi * 1024.f, er, ei);
      *(f32x4*)(par + lane * 8) = f32x4{zs, zi, wr, wi};
      *(f32x4*)(par + lane * 8 + 4) = f32x4{qr, qi, er, ei};
      const float p2r = er * er - ei * ei, p2i = 2.f * er * ei;
      const float k2048 = wave_sum(wr * p2r - wi * p2i) * 16384.f;
      STW(3, 0, 4);
      const int r = lane & 15, g = lane >> 4;
      const float va = 8.f * r;
      const int off = r < 8 ? r : r < 12 ? 256 + 2 * (r - 8) : 512 + 256 * ((r - 12) >> 1) + 4 * (r & 1);
      const float vb = (float)off;
      f32x4 acc[2][2];
#pragma unroll
      for (int t = 0; t < 4; ++t) acc[t >> 1][t & 1] = f32x4{0.f, 0.f, 0.f, 0.f};
#pragma unroll 1
      for (int ks = 0; ks < 4; ++ks) {
        u32x4 a0, a1, b0, b1;
#pragma unroll
        for (int j = 0; j < 4; ++j) {
          const int n = ks * 16 + g * 4 + j;
          const f32x4 pp = *(const f32x4*)(par + n * 8), pq = *(const f32x4*)(par + n * 8 + 4);
          float tr, ti, xr_, xi_;
          cexp2(__builtin_fmaf(pp[0], va, 14.f), pp[1] * va, tr, ti);
          cexp2(pp[0] * vb, pp[1] * vb, xr_, xi_);
          const float ur = pp[2] * xr_ - pp[3] * xi_, ui = pp[2] * xi_ + pp[3] * xr_;
          a0[j] = pk_f16(tr, ti);
          a1[j] = pk_f16(tr * pq[0] - ti * pq[1], tr * pq[1] + ti * pq[0]);
          b0[j] = pk_f16(ur, -ui);
          b1[j] = pk_f16(ur * pq[2] - ui * pq[3], -(ur * pq[3] + ui * pq[2]));
        }
        acc[0][0] = __builtin_amdgcn_mfma_f32_16x16x32_f16(__builtin_bit_cast(f16x8, a0), __builtin_bit_cast(f16x8, b0), acc[0][0], 0, 0, 0);
        acc[1][0] = __builtin_amdgcn_mfma_f32_16x16x32_f16(__builtin_bit_cast(f16x8, a1), __builtin_bit_cast(f16x8, b0), acc[1][0], 0, 0, 0);
        acc[0][1] = __builtin_amdgcn_mfma_f32_16x16x32_f16(__builtin_bit_cast(f16x8, a0), __builtin_bit_cast(f16x8, b1), acc[0][1], 0, 0, 0);
        acc[1][1] = __builtin_amdgcn_mfma_f32_16x16x32_f16(__builtin_bit_cast(f16x8, a1), __builtin_bit_cast(f16x8, b1), acc[1][1], 0, 0, 0);
      }
      STW(3, 1, 4);
      float kl = k2048 * osc;
#pragma unroll
      for (int t = 0; t < 4; ++t) acc[t >> 1][t & 1] *= osc;
      if (zmax > 0.f) {
#pragma unroll
        for (int t = 0; t < 4; ++t)
#pragma unroll
          for (int e = 0; e < 4; ++e)
            acc[t >> 1][t & 1][e] *= __builtin_amdgcn_exp2f(zmax * (vb + (float)(8 * (16 * (t >> 1) + e) + 1024 * (t & 1)) + 32.f * g));
        kl *= __builtin_amdgcn_exp2f(zmax * 2048.f);
      }
      {
        const int stride = r < 8 ? 8 : r < 12 ? 4 : 2;
        const int base = r < 8 ? r : r < 12 ? 256 + (r - 8) : 384 + 64 * ((r - 12) >> 1) + (r & 1);
        float* p0 = tabf + (base + stride * 4 * g) * 2 + hw;
#pragma unroll
        for (int it = 0; it < 2; ++it)
#pragma unroll
          for (int e = 0; e < 4; ++e) p0[stride * (16 * it + e) * 2] = acc[it][0][e];
        if ((off & 255) == 0) {
          float* p1 = tabf + (512 + (off >> 3) + 4 * g) * 2 + hw;
#pragma unroll
          for (int it = 0; it < 2; ++it)
#pragma unroll
            for (int e = 0; e < 4; ++e) p1[(16 * it + e) * 2] = acc[it][1][e];
        }
        if (lane < 2) tabf[(640 + lane) * 2 + hw] = lane ? 0.f : kl;
      }
      grp_arrive(xb, j4, 1, lane);
      const int l2 = hw * 64 + lane;
      char* lk = lds + X_KT + ch * KT_B;
      int ix[17];
#pragma unroll
      for (int e = 0; e < 17; e += 2) {
        const int j0 = 2047 - 16 * l2 - e, j1 = j0 - 1;
        const unsigned bb = pk_bf16((float)j0, (float)j1);
        const int i0 = (int)(bb & 0xffff) - (0x4300 - 128), i1 = (int)(bb >> 16) - (0x4300 - 128);
        ix[e] = j0 < 128 ? (j0 < 0 ? 641 : j0) : i0;
        if (e + 1 < 17) ix[e + 1] = j1 < 128 ? j1 : i1;
      }
      if (l2 < 8) {
        const int pc = (l2 & 2) ? 258 + (l2 & 1) : (l2 & 1);
        *(u32x4*)(lk + (l2 >> 2) * R1OFF + pc * 16) = u32x4{0, 0, 0, 0};
      }
      if (j4 == 0) *(u32x4*)(lds + X_U + (lane & 1) * U_B + (lane >> 1) * 16) = u32x4{0, 0, 0, 0};
#pragma unroll
      for (int e = 0; e < 17; ++e) asm volatile("" : "+v"(ix[e]));
      grp_wait<0>(xb, 1);
      f32x2 tv[17];
#pragma unroll
      for (int e = 0; e < 17; ++e) tv[e] = *(const f32x2*)(tabf + ix[e] * 2);
      __builtin_amdgcn_sched_barrier(0);
      float val[17];
#pragma unroll
      for (int e = 0; e < 17; ++e) val[e] = tv[e][0] + tv[e][1];
#pragma unroll
      for (int hf = 0; hf < 2; ++hf) {
        u32x4 o0, o1;
#pragma unroll
        for (int e = 0; e < 4; ++e) { o0[e] = pk_bf16(val[8 * hf + 2 * e], val[8 * hf + 2 * e + 1]); o1[e] = pk_bf16(val[8 * hf + 2 * e + 1], val[8 * hf + 2 * e + 2]); }
        *(u32x4*)(lk + 32 + l2 * 32 + hf * 16) = o0;
        *(u32x4*)(lk + R1OFF + 32 + l2 * 32 + hf * 16) = o1;
      }
      if (l2 == 0) *(unsigned*)(lk + R1OFF + 28) = pk_bf16(0.f, val[0]);
      STW(3, 2, 4);
    }
    word_wait(sy + 11, 1);
    STW(3, 3, 4);
    {
      u32x4 u0, u1;
      const int t0 = j4 * 64 + (lane >> 1);
      asm volatile("global_load_dwordx4 %0, %2, off sc1\n\tglobal_load_dwordx4 %1, %3, off sc1\n\ts_waitcnt vmcnt(0)" : "=&v"(u0), "=&v"(u1)
                   : "v"(a.X2 + ((size_t)t0 * 512 + tb * 2 + (lane & 1)) * 16), "v"(a.X2 + ((size_t)(t0 + 32) * 512 + tb * 2 + (lane & 1)) * 16) : "memory");
      char* lu = lds + X_U + (lane & 1) * U_B;
      *(u32x4*)(lu + UPAD * 2 + t0 * 16) = u0;
      *(u32x4*)(lu + UPAD * 2 + (t0 + 32) * 16) = u1;
    }
    grp_bar(xb, j4, 2, lane);
    if (b == 0 && tid == 256) st4_dev(a.fl, tag);
    STW(3, 8, 4);
    {
      const float dsk = bf_lo(praw[7]);
      const char* lk = lds + X_KT + ch * KT_B;
      const char* lu = lds + X_U + ch * U_B;
      const int col = lane & 15, g = lane >> 4;
      const int p00 = 2047 - col + 8 * g;
      const char* gbase = lk + ((p00 & 1) ? R1OFF : 0) + (p00 >> 1) * 4 - 256 * hw;
      const char* bbase = lu + (UPAD - 16 + 16 * col + 8 * g) * 2 - 256 * hw;
      f32x4 acc[8];
#pragma unroll
      for (int i = 0; i < 8; ++i) acc[i] = f32x4{0.f, 0.f, 0.f, 0.f};
      bf16x8 bf[2][8], gf[2][8];
#define CLD(set, rr) do { _Pragma("unroll") for (int i = 0; i < 8; ++i) { \
          bf[set][i] = *(const bf16x8*)(bbase + 64 * (8 * i - (rr))); \
          const unsigned* gp = (const unsigned*)(gbase - 64 * (8 * i + (rr))); \
          u32x4 gv = {gp[0], gp[1], gp[2], gp[3]}; gf[set][i] = __builtin_bit_cast(bf16x8, gv); } } while (0)
      CLD(0, 0);
      CLD(1, 1);
      if (CGATE == 1 && has1) word_wait<2>(sy + 9, 1);
      if (CGATE == 2 && has1) grp_wait<1>(sy + 24, 1);
      STW(3, 4, 4);
#pragma unroll
      for (int rr = 0; rr < 4; ++rr) {
#pragma unroll
        for (int bq = 0; bq < 8; ++bq)
#pragma unroll
          for (int i = 0; i + bq < 8; ++i) acc[i + bq] = __builtin_amdgcn_mfma_f32_16x16x32_bf16(gf[rr & 1][bq], bf[rr & 1][i], acc[i + bq], 0, 0, 0);
        asm volatile("" : "+v"(acc[0]), "+v"(acc[1]), "+v"(acc[2]), "+v"(acc[3]), "+v"(acc[4]), "+v"(acc[5]), "+v"(acc[6]), "+v"(acc[7]) :: "memory");
        if (rr + 2 < 4) CLD(rr & 1, rr + 2);
        __builtin_amdgcn_sched_barrier(0);
      }
#undef CLD
      __builtin_amdgcn_s_setprio(XPR);
      STW(3, 5, 4);
      float* xc = (float*)(lds + X_SC + ch * 8192);
      f32x4 keep[4];
#pragma unroll
      for (int i = 0; i < 4; ++i) {
        f32x4 give;
#pragma unroll
        for (int e = 0; e < 4; ++e) { give[e] = hw ? acc[2 * i][e] : acc[2 * i + 1][e]; keep[i][e] = hw ? acc[2 * i + 1][e] : acc[2 * i][e]; }
        *(f32x4*)(xc + ((hw * 4 + i) * 64 + lane) * 4) = give;
      }
      grp_bar<0>(xb, j4, 3, lane);
      u32x2 o[4];
#pragma unroll
      for (int i = 0; i < 4; ++i) {
        const f32x4 y = keep[i] + *(const f32x4*)(xc + (((hw ^ 1) * 4 + i) * 64 + lane) * 4);
        const int t = 256 * (2 * i + hw) + 16 * col + 4 * g;
        const u32x2 uu = *(const u32x2*)(lu + (UPAD + t) * 2);
        o[i] = u32x2{pk_bf16(y[0] + bf_lo(uu[0]) * dsk, y[1] + bf_hi(uu[0]) * dsk), pk_bf16(y[2] + bf_lo(uu[1]) * dsk, y[3] + bf_hi(uu[1]) * dsk)};
      }
#pragma unroll
      for (int ip = 0; ip < 2; ++ip) {
        u32x4 q4;
#pragma unroll
        for (int e = 0; e < 2; ++e) {
          auto sw = __builtin_amdgcn_permlane16_swap(o[2 * ip][e], o[2 * ip + 1][e], false, false);
          q4[e] = sw[0]; q4[2 + e] = sw[1];
        }
        const int t = 256 * (2 * (2 * ip + (g & 1)) + hw) + 16 * col + 8 * (g >> 1);
        st16_dev(a.sT + ((size_t)d * L_ + t) * 2, q4);
      }
      asm volatile("s_waitcnt vmcnt(0)" ::: "memory");
      grp_bar<1>(xb, j4, 4, lane);
      if (tid == 256) st4_dev(a.fl + FL_S + tb * GSP, tag);
      STW(3, 6, 4);
    }
    {
      if (j4 == 0) {
        word_wait<2>(sy + 11, 2);
        __builtin_amdgcn_s_sleep(SDEL);
        poll_flags<SBK>(a.fl + FL_S + (dT2 * 32 + (lane & 31)) * GSP, tag, lane >= 32);
        if (lane == 0) sy[17] = 1;
      } else word_wait<2>(sy + 17, 1);
      u32x4 s0, s1;
      const int rw_ = j4 * 16 + (lane >> 3);
      const char* sp = a.sT + ((size_t)(dT2 * 64 + rw_) * L_ + sTl * 64) * 2 + (lane & 7) * 16;
      asm volatile("global_load_dwordx4 %0, %2, off sc1\n\tglobal_load_dwordx4 %1, %3, off sc1\n\ts_waitcnt vmcnt(0)" : "=&v"(s0), "=&v"(s1)
                   : "v"(sp), "v"(sp + (size_t)8 * L_ * 2) : "memory");
      *(u32x4*)(lds + X_SC + rw_ * 144 + (lane & 7) * 16) = s0;
      *(u32x4*)(lds + X_SC + (rw_ + 8) * 144 + (lane & 7) * 16) = s1;
      grp_bar(xb, j4, 5, lane);
      if (tid == 256) sy[10] = 1;
      STW(3, 7, 4);
      STDUMP(4);
    }
  }
}

__device__ __attribute__((noinline)) void code_end_marker() { asm volatile("s_nop 0"); }

extern "C" void kernel_launch(void* const* d_in, const int* in_sizes, int n_in, void* d_out, int out_size, void* d_ws,
                              size_t ws_size, hipStream_t stream) {
  char* ws = (char*)d_ws;
  Args a;
  a.x = (const u32x4*)d_in[0];
  a.Are = (cus)d_in[1]; a.Aim = (cus)d_in[2]; a.Bre = (cus)d_in[3]; a.Bim = (cus)d_in[4]; a.Cre = (cus)d_in[5]; a.Cim = (cus)d_in[6];
  a.logdt = (cus)d_in[7]; a.Dskip = (cus)d_in[8];
  a.w1 = (cus)d_in[10]; a.b1 = (cus)d_in[11]; a.w2 = (cus)d_in[12]; a.b2 = (cus)d_in[13];
  a.out = (unsigned short*)d_out;
  a.X2 = ws; a.Hf = ws + (2u << 20); a.Gf = ws + (4u << 20); a.sT = ws + (6u << 20); a.fl = (unsigned*)(ws + (8u << 20));
  (void)hipFuncSetAttribute((const void*)k_mega, hipFuncAttributeMaxDynamicSharedMemorySize, LDS_B);
  k_mega<<<256, 512, LDS_B, stream>>>(a STPASS);
}
```

```cpp
#include <hip/hip_runtime.h>
#include <hip/hip_bf16.h>
#include <stdint.h>

typedef short s16x4 __attribute__((ext_vector_type(4)));
typedef short bf16x8 __attribute__((ext_vector_type(8)));
typedef _Float16 f16x8 __attribute__((ext_vector_type(8)));
typedef _Float16 h2_t __attribute__((ext_vector_type(2)));
typedef __bf16 bf2_t __attribute__((ext_vector_type(2)));
typedef float f32x2 __attribute__((ext_vector_type(2)));
typedef float f32x4 __attribute__((ext_vector_type(4)));
typedef unsigned u32x2 __attribute__((ext_vector_type(2)));
typedef unsigned u32x4 __attribute__((ext_vector_type(4)));
typedef const unsigned short* cus;
#define DI __device__ __forceinline__
#define LDSP __attribute__((address_space(3)))

#define STW(k, slot, wv)
#define STDECL
#define STDUMP(wv)
#define STARG
#define STPASS
#define RW1 5
#define RW2 4
#define WL 7
#define AD 3
#define PBK 6
#define CGATE 2
#define IDEL 550
#define SDEL 32
#define SBK 8
#define HSP 1
#define GSP 32
#define HDEL 8
#define PSTEP 6
#define XPR 3
#define PFA 3
#define E1 0
#define E2 0
#define XPOLL 0
#define GLATE 1
#define E0 0
#define TSPL 0
#define SPF 0
#define APOL " sc1"
constexpr int L_ = 2048, D_ = 512, N_ = 128, F_ = 1792;
constexpr int TABN = 648;
constexpr float LOG2E = 1.44269504088896341f;
constexpr int SPIN_CAP = 1 << 18;

DI unsigned pk_bf16(float lo, float hi) {
  f32x2 f = {lo, hi};
  return __builtin_bit_cast(unsigned, __builtin_convertvector(f, bf2_t));
}
DI unsigned pk_f16(float lo, float hi) {
  f32x2 f = {lo, hi};
  return __builtin_bit_cast(unsigned, __builtin_convertvector(f, h2_t));
}
DI float bf_lo(unsigned u) { return __uint_as_float(u << 16); }
DI float bf_hi(unsigned u) { return __uint_as_float(u & 0xffff0000u); }
DI float rbf(float x) { return bf_lo(pk_bf16(x, 0.f)); }
DI float bf2f(unsigned short u) { return __uint_as_float(((unsigned)u) << 16); }

template <int CTRL, int RM, bool BC> DI float dppf(float v) {
  return __builtin_bit_cast(float, __builtin_amdgcn_update_dpp(0, __builtin_bit_cast(int, v), CTRL, RM, 0xf, BC));
}
DI float wave_sum(float v) {
  v += dppf<0x111, 0xf, true>(v);
  v += dppf<0x112, 0xf, true>(v);
  v += dppf<0x114, 0xf, true>(v);
  v += dppf<0x118, 0xf, true>(v);
  v += dppf<0x142, 0xa, false>(v);
  v += dppf<0x143, 0xc, false>(v);
  return __builtin_bit_cast(float, __builtin_amdgcn_readlane(__builtin_bit_cast(int, v), 63));
}
DI float wave_max_nn(float v) {
  v = fmaxf(v, dppf<0x111, 0xf, true>(v));
  v = fmaxf(v, dppf<0x112, 0xf, true>(v));
  v = fmaxf(v, dppf<0x114, 0xf, true>(v));
  v = fmaxf(v, dppf<0x118, 0xf, true>(v));
  v = fmaxf(v, dppf<0x142, 0xa, false>(v));
  v = fmaxf(v, dppf<0x143, 0xc, false>(v));
  return __builtin_bit_cast(float, __builtin_amdgcn_readlane(__builtin_bit_cast(int, v), 63));
}
DI void touch(unsigned ldum, const void* p) { asm volatile("s_mov_b32 m0, %0\n\ts_nop 0\n\tglobal_load_lds_dword %1, off" :: "s"(ldum), "v"(p) : "memory"); }
DI void lds_barrier() { asm volatile("s_waitcnt lgkmcnt(0)" ::: "memory"); __builtin_amdgcn_s_barrier(); asm volatile("" ::: "memory"); }
DI void st16_dev(void* p, u32x4 v) { asm volatile("global_store_dwordx4 %0, %1, off sc1\n\ts_nop 1" :: "v"(p), "v"(v) : "memory"); }
DI void st8_dev(void* p, u32x2 v) { asm volatile("global_store_dwordx2 %0, %1, off sc1" :: "v"(p), "v"(v) : "memory"); }
DI void st4_dev(void* p, unsigned v) { asm volatile("global_store_dword %0, %1, off sc1" :: "v"(p), "v"(v) : "memory"); }
DI unsigned ld4_dev_wait(const void* p) { unsigned r; asm volatile("global_load_dword %0, %1, off sc1\n\ts_waitcnt vmcnt(0)" : "=&v"(r) : "v"(p) : "memory"); return r; }

DI void grp_arrive(volatile LDSP unsigned* words, int idx, unsigned ph, int lane) {
  asm volatile("s_waitcnt lgkmcnt(0)" ::: "memory");
  if (lane == 0) words[idx] = ph;
}
template <int SL = 1>
DI void grp_wait(volatile LDSP unsigned* words, unsigned ph) {
#pragma unroll 1
  for (int sp = 0; sp < SPIN_CAP * 8; ++sp) {
    const u32x4 v = *(volatile LDSP u32x4*)words;
    const unsigned m = min(min(v[0], v[1]), min(v[2], v[3]));
    if (__builtin_amdgcn_readfirstlane(m) >= ph) break;
    if (SL) __builtin_amdgcn_s_sleep(SL);
  }
  asm volatile("" ::: "memory");
}
template <int SL = 1>
DI void grp_bar(volatile LDSP unsigned* words, int idx, unsigned ph, int lane) { grp_arrive(words, idx, ph, lane); grp_wait<SL>(words, ph); }
template <int SL = 1>
DI void word_wait(volatile LDSP unsigned* word, unsigned v) {
#pragma unroll 1
  for (int sp = 0; sp < SPIN_CAP * 8; ++sp) {
    if (__builtin_amdgcn_readfirstlane(*word) >= v) break;
    if (SL) __builtin_amdgcn_s_sleep(SL);
  }
  asm volatile("" ::: "memory");
}
template <int BK = PBK>
DI void poll_flags(const unsigned* p, unsigned tag, bool done) {
#pragma unroll 1
  for (int sp = 0; sp < SPIN_CAP; ++sp) {
    if (!done) done = ld4_dev_wait(p) == tag;
    if (__all(done)) break;
    __builtin_amdgcn_s_sleep(BK);
  }
}
DI void poll_flags4(const unsigned* p, unsigned tag) {
  bool done = false;
#pragma unroll 1
  for (int sp = 0; sp < SPIN_CAP; ++sp) {
    if (!done) {
      u32x4 f;
      asm volatile("global_load_dwordx4 %0, %1, off sc1\n\ts_waitcnt vmcnt(0)" : "=&v"(f) : "v"(p) : "memory");
      done = f[0] == tag && f[1] == tag && f[2] == tag && f[3] == tag;
    }
    if (__all(done)) break;
    __builtin_amdgcn_s_sleep(PBK);
  }
}

DI int kpos(int kb) { return kb ^ (((kb >> 2) & 1) << 1); }

template <bool PIVOT>
DI void ln8(float (&v)[8], u32x4& o) {
  const float p = PIVOT ? __builtin_bit_cast(float, __builtin_amdgcn_readfirstlane(__builtin_bit_cast(int, v[0]))) : 0.f;
  float s0 = 0.f, s1 = 0.f, q0 = 0.f, q1 = 0.f;
#pragma unroll
  for (int i = 0; i < 4; ++i) { const float t = v[i] - p, u = v[i + 4] - p; s0 += t; s1 += u; q0 = __builtin_fmaf(t, t, q0); q1 = __builtin_fmaf(u, u, q1); }
  const float ds = wave_sum(s0 + s1) * (1.f / D_), dq = wave_sum(q0 + q1) * (1.f / D_);
  const float mean = p + ds, var = fmaxf(__builtin_fmaf(-ds, ds, dq), 0.f);
  const float mb = rbf(mean), vb = rbf(var);
  const float den = rbf(__builtin_amdgcn_sqrtf(rbf(vb + 1e-5f)));
  const float inv = __builtin_amdgcn_rcpf(den);
#pragma unroll
  for (int i = 0; i < 4; ++i) {
    const unsigned t = pk_bf16(v[2 * i] - mb, v[2 * i + 1] - mb);
    o[i] = pk_bf16(bf_lo(t) * inv, bf_hi(t) * inv);
    v[2 * i] = bf_lo(o[i]); v[2 * i + 1] = bf_hi(o[i]);
  }
}
DI void cexp2(float le, float ph, float& re, float& im) {
  const float e = __builtin_amdgcn_exp2f(le);
  const float p = __builtin_amdgcn_fractf(ph);
  re = e * __builtin_amdgcn_cosf(p);
  im = e * __builtin_amdgcn_sinf(p);
}
DI float gelu_tanh(float x) {
  const float u = x * (1.f + 0.044715f * x * x);
  return x * __builtin_amdgcn_rcpf(1.f + __builtin_amdgcn_exp2f(-2.f * 0.7978845608028654f * LOG2E * u));
}
DI float silu(float x) { return x * __builtin_amdgcn_rcpf(1.f + __builtin_amdgcn_exp2f(-LOG2E * x)); }

#define DMA4(ldsaddr, vo, sb) do { unsigned keep_; \
    asm volatile("s_mov_b32 %0, m0\n\ts_mov_b32 m0, %1\n\ts_nop 0\n\t" \
                 "global_load_lds_dwordx4 %2, %6\n\tglobal_load_lds_dwordx4 %3, %6 offset:1024\n\t" \
                 "global_load_lds_dwordx4 %4, %6 offset:2048\n\tglobal_load_lds_dwordx4 %5, %6 offset:3072\n\ts_mov_b32 m0, %0" \
                 : "=&s"(keep_) : "s"(__builtin_amdgcn_readfirstlane(ldsaddr)), "v"(vo[0]), "v"(vo[1]), "v"(vo[2]), "v"(vo[3]), "s"(sb) : "memory"); } while (0)
template <int RB> DI void w_offsets(int lane, unsigned (&vo)[4]) {
  const int lr = lane >> 3, p8 = lane & 7;
#pragma unroll
  for (int j = 0; j < 4; ++j) vo[j] = (8 * j + lr) * RB + ((p8 ^ ((lr & 2) | ((j & 1) << 2))) << 4) - 1024 * j;
}
struct TrAddr { const char* p[4]; };
DI void tr_addr(TrAddr& t, const char* ring, int lane) {
  const int g = lane >> 4, q = (lane & 15) >> 2, p = lane & 3;
  const int frow = (q & 2) | ((g & 1) << 2);
  const char* trb = ring + (8 * g + q) * 128 + (p & 1) * 8;
#pragma unroll
  for (int j = 0; j < 4; ++j) t.p[j] = trb + (((2 * j + (p >> 1)) ^ frow) << 4);
}
DI void tr_read(bf16x8 (&fr)[4], const TrAddr& t, int off) {
#pragma unroll
  for (int j = 0; j < 4; ++j) {
    s16x4 lo = __builtin_amdgcn_ds_read_tr16_b64_v4i16((LDSP s16x4*)(t.p[j] + off));
    s16x4 hi = __builtin_amdgcn_ds_read_tr16_b64_v4i16((LDSP s16x4*)(t.p[j] + off + 512));
    fr[j] = __builtin_shufflevector(lo, hi, 0, 1, 2, 3, 4, 5, 6, 7);
  }
}
template <int RB, int OFF>
DI void w_issue(const char* wb, const unsigned (&vo)[4], unsigned lring, int k0, int k1) {
#pragma unroll 1
  for (int k = k0; k < k1; ++k) DMA4(lring + ((k + OFF) % WL) * 4096, vo, wb + (size_t)k * 128 * RB);
}
template <int RWN, int OFF>
DI void w_to_regs(const TrAddr& tra, bf16x8 (&wr)[RWN][4]) {
#pragma unroll
  for (int k = 0; k < RWN; ++k) tr_read(wr[k], tra, ((k + OFF) % WL) * 4096);
#pragma unroll
  for (int k = 0; k < RWN; ++k) asm volatile("s_waitcnt lgkmcnt(0)" : "+v"(wr[k][0]), "+v"(wr[k][1]), "+v"(wr[k][2]), "+v"(wr[k][3]) :: "memory");
}
template <int NKB, int RWN, int OFF> struct Sch {
  static constexpr int NL = NKB - RWN;
  static constexpr int blk(int i) { return i < NL ? RWN + i : i - NL; }
  static constexpr int slot(int i) { return (RWN + i + OFF) % WL; }
  static constexpr bool dma(int s) { return s >= 1 && s - 1 + WL < NL; }
  static constexpr int younger(int i) {
    int pos = 0, pa = -1, pw = -1;
    for (int k = 0; k < AD; ++k) { if (k == i) pa = pos; ++pos; }
    for (int s = 0; s < i; ++s) {
      if (dma(s)) { if (s - 1 + WL == i + 1) pw = pos; ++pos; }
      if (s + AD < NKB) { if (s + AD == i) pa = pos; ++pos; }
    }
    const int need = pa > pw ? pa : pw;
    return pos - 1 - need;
  }
};
DI bf16x8 tr_frag(const char* p) {
  s16x4 lo = __builtin_amdgcn_ds_read_tr16_b64_v4i16((LDSP s16x4*)(p));
  s16x4 hi = __builtin_amdgcn_ds_read_tr16_b64_v4i16((LDSP s16x4*)(p + 512));
  return __builtin_shufflevector(lo, hi, 0, 1, 2, 3, 4, 5, 6, 7);
}
template <int NKB, int RB, bool SWAP, int RWN, int OFF>
DI void gemm_main(const char* ab, const char* abo, const char* wb, const unsigned (&vo)[4], unsigned lring, const TrAddr& tra,
                  bf16x8 (&wr)[RWN][4], int w, int lane, f32x4 (&acc)[4][4], char* park, volatile LDSP unsigned* sig, unsigned long long* lst = nullptr) {
  typedef Sch<NKB, RWN, OFF> S;
  unsigned va[4];
#pragma unroll
  for (int t = 0; t < 4; ++t) va[t] = lane * 16 + ((t + w + 1) & 3) * 1024;
  u32x4 av[AD][4];
  bf16x8 fr[4];
#define ABASE(i) (((S::blk(i) & 1) ? abo : ab) + (size_t)S::blk(i) * 16384)
#define LDA1(i, t) asm volatile("global_load_dwordx4 %0, %1, %2" APOL : "=&v"(av[(i) % AD][t]) : "v"(va[t]), "s"(ABASE(i)) : "memory")
#define DMA1(i, q) asm volatile("s_mov_b32 m0, %0\n\ts_nop 0\n\tglobal_load_lds_dwordx4 %1, %2 offset:%3" \
                 :: "s"(__builtin_amdgcn_readfirstlane(lring + S::slot((i) - 1) * 4096)), "v"(vo[q]), "s"(wb + (size_t)(RWN + (i) - 1 + WL) * 128 * RB), "i"((q) * 1024) : "memory")
#pragma unroll
  for (int i = 0; i < AD; ++i) { LDA1(i, 0); LDA1(i, 1); LDA1(i, 2); LDA1(i, 3); }
  asm volatile("s_waitcnt vmcnt(%0)" :: "i"(4 * AD) : "memory");
#pragma unroll
  for (int j = 0; j < 4; ++j) fr[j] = tr_frag(tra.p[j] + S::slot(0) * 4096);
#pragma unroll
  for (int i = 0; i < NKB; ++i) {
    const int st = i % AD;
    asm volatile("s_waitcnt vmcnt(%4)" : "+v"(av[st][0]), "+v"(av[st][1]), "+v"(av[st][2]), "+v"(av[st][3]) : "i"(4 * S::younger(i)) : "memory");
    if (SWAP && i == PSTEP && lane == 0) *sig = 1;
    if (i == NKB - 1) {
#pragma unroll
      for (int m = 0; m < 16; ++m) {
        const int t = m >> 2, j = m & 3;
        if (SWAP) asm volatile("v_mfma_f32_16x16x32_bf16 %0, %1, %2, %0" : "+v"(acc[j][t]) : "v"(av[st][t]), "v"(wr[RWN - 1][j]) : "memory");
        else asm volatile("v_mfma_f32_16x16x32_bf16 %0, %1, %2, %0" : "+v"(acc[j][t]) : "v"(wr[RWN - 1][j]), "v"(av[st][t]) : "memory");
        if (m >= 8) *(f32x4*)(park + (m - 8) * 1024) = acc[(m - 8) & 3][(m - 8) >> 2];
      }
      continue;
    }
#pragma unroll
    for (int j = 0; j < 4; ++j) {
#pragma unroll
      for (int t = 0; t < 4; ++t) {
        const bf16x8 wf = i < S::NL ? fr[j] : wr[i < S::NL ? 0 : i - S::NL][j];
        if (i == 0) {
          if (SWAP) asm volatile("v_mfma_f32_16x16x32_bf16 %0, %1, %2, 0" : "=&v"(acc[j][t]) : "v"(av[st][t]), "v"(wf) : "memory");
          else asm volatile("v_mfma_f32_16x16x32_bf16 %0, %1, %2, 0" : "=&v"(acc[j][t]) : "v"(wf), "v"(av[st][t]) : "memory");
        } else {
          if (SWAP) asm volatile("v_mfma_f32_16x16x32_bf16 %0, %1, %2, %0" : "+v"(acc[j][t]) : "v"(av[st][t]), "v"(wf) : "memory");
          else asm volatile("v_mfma_f32_16x16x32_bf16 %0, %1, %2, %0" : "+v"(acc[j][t]) : "v"(wf), "v"(av[st][t]) : "memory");
        }
        if (S::dma(i) && j < 2 && (t == 0 || t == 2)) { if (j == 0 && t == 0) DMA1(i, 0); else if (j == 0) DMA1(i, 1); else if (t == 0) DMA1(i, 2); else DMA1(i, 3); }
        if (j == 3 && i + AD < NKB) { if (t == 0) LDA1(i + AD, 0); else if (t == 1) LDA1(i + AD, 1); else if (t == 2) LDA1(i + AD, 2); else LDA1(i + AD, 3); }
      }
      if (i + 1 < S::NL) fr[j] = tr_frag(tra.p[j] + S::slot(i + 1) * 4096);
    }
  }
#pragma unroll
  for (int j = 0; j < 4; ++j) { *(f32x4*)(park + (8 + j) * 1024) = acc[j][2]; asm volatile("" ::: "memory"); }
  asm volatile("s_nop 7\n\ts_nop 7" ::: "memory");
#undef LDA1
#undef DMA1
#undef ABASE
}
DI void reduce4(f32x4 (&acc)[4][4], f32x4 (&res)[4], char* lds, int w, int lane, volatile LDSP unsigned* words, unsigned ph) {
  grp_bar<SPF>(words, w, ph, lane);
  f32x4 pv[3][4];
#pragma unroll
  for (int k = 1; k < 4; ++k) {
    const char* p = lds + ((w + k) & 3) * (WL * 4096) + (3 - k) * 4096 + lane * 16;
#pragma unroll
    for (int j = 0; j < 4; ++j) pv[k - 1][j] = *(const f32x4*)(p + j * 1024);
  }
#pragma unroll
  for (int j = 0; j < 4; ++j) res[j] = (acc[j][3] + pv[0][j]) + (pv[1][j] + pv[2][j]);
}

struct Args {
  const u32x4* x; cus Are, Aim, Bre, Bim, Cre, Cim, logdt, Dskip, w1, b1, w2, b2;
  unsigned short* out;
  char* Hf;  char* X2; char* Gf; char* sT; unsigned* fl;
};
constexpr int FL_H = 64, FL_G = FL_H + 256 * 32, FL_S = FL_G + 256 * 32;

constexpr int KR = 2080;
constexpr int UPAD = 256;
constexpr int R1OFF = KR * 2 + 128;
constexpr int KT_B = R1OFF + KR * 2 + 32;
constexpr int U_B = (UPAD + L_) * 2;
constexpr int XR = 4 * WL * 4096;
constexpr int X_KT = XR, X_U = X_KT + 2 * KT_B, X_SC = X_U + 2 * U_B;
constexpr int SC_B = 8192 + 2 * TABN * 8;
constexpr int X_SY = X_SC + SC_B;
constexpr int X_B1 = X_SY + 128;
constexpr int X_DUM = X_B1 + 512;
constexpr int LDS_B = X_DUM + 256;
static_assert(LDS_B <= 160 * 1024 && SC_B >= 16384 && 2 * KT_B >= 16 * (1024 + 32), "LDS");

__device__ __attribute__((noinline)) void code_end_marker();

__global__ __launch_bounds__(512, 2) void k_mega(Args a STARG) {
  extern __shared__ __attribute__((aligned(16))) char lds[];
  const int tid = threadIdx.x, lane = tid & 63, w = __builtin_amdgcn_readfirstlane(tid >> 6);
  const int b = blockIdx.x, xg = b & 7, r_ = b >> 3;
  const int tb = xg * 32 + r_;
  const unsigned lbase = (unsigned)(size_t)(LDSP char*)lds;
  volatile LDSP unsigned* sy = (volatile LDSP unsigned*)(lds + X_SY);
  STDECL
  STW(0, 0, 0);
  const bool has1 = r_ < 28;
  const int idx1 = xg * 28 + (has1 ? r_ : 0);
  const int fT = idx1 >> 3, dT = idx1 & 7;
  const int sTl = tb >> 3, dT2 = tb & 7;
  u32x4 xr; unsigned ep;
  asm volatile("global_load_dwordx4 %0, %2, %3\n\tglobal_load_dword %1, %4, %5 sc1" : "=&v"(xr), "=&v"(ep)
               : "v"(lane * 16), "s"(a.x + (size_t)(tb * 8 + w) * 64), "v"(0), "s"(a.fl) : "memory");
  if (tid < 32) sy[tid] = 0;
  unsigned praw[8];
  if (w >= 4) {
    const int i = (tb * 2) * N_ + (w - 4) * 64;
    const int dch = tb * 2 + ((w - 4) >> 1);
    asm volatile("global_load_ushort %0, %8, %10\n\tglobal_load_ushort %1, %8, %11\n\tglobal_load_ushort %2, %8, %12\n\tglobal_load_ushort %3, %8, %13\n\t"
                 "global_load_ushort %4, %8, %14\n\tglobal_load_ushort %5, %8, %15\n\tglobal_load_ushort %6, %9, %16\n\tglobal_load_ushort %7, %9, %17\n\ts_barrier"
                 : "=&v"(praw[0]), "=&v"(praw[1]), "=&v"(praw[2]), "=&v"(praw[3]), "=&v"(praw[4]), "=&v"(praw[5]), "=&v"(praw[6]), "=&v"(praw[7])
                 : "v"(lane * 2), "v"(0), "s"(a.Are + i), "s"(a.Aim + i), "s"(a.Bre + i), "s"(a.Bim + i), "s"(a.Cre + i), "s"(a.Cim + i),
                   "s"(a.logdt + dch), "s"(a.Dskip + dch) : "memory");
    st16_dev(a.sT + (size_t)tb * 8192 + (w - 4) * 2048 + lane * 16, u32x4{~0u, ~0u, ~0u, ~0u});
    st16_dev(a.sT + (size_t)tb * 8192 + (w - 4) * 2048 + 1024 + lane * 16, u32x4{~0u, ~0u, ~0u, ~0u});
    const int li = r_ * 256 + tid - 256, ft0 = (xg * 28) >> 3;
    const char* t1 = (const char*)a.w1 + (size_t)(li >> 2) * (F_ * 2) + (ft0 + (li & 3)) * 128;
    if (TSPL == 1) { const int l1 = r_ * 128 + ((tid - 256) & 127); t1 = (const char*)a.w1 + (size_t)(l1 >> 2) * (F_ * 2) + (ft0 + (l1 & 3)) * 128; }
    if (TSPL == 2) t1 = (const char*)a.fl;
    const char* t2 = t1;
    {
      unsigned long long pc;
      asm volatile("s_getpc_b64 %0" : "=s"(pc));
      const unsigned long long pe = (unsigned long long)(void*)&code_end_marker;
      const unsigned long long pa = (pc & ~127ull) + (unsigned long long)(((w - 4) * 4 + (r_ & 3)) * 64 + lane) * 128;
      if (pe > pc && pe - pc < 65536 && pa + 4 <= pe && r_ >= 28 && w < 6) t2 = (const char*)pa;
    }
    asm volatile("s_mov_b32 m0, %2\n\ts_nop 0\n\tglobal_load_lds_dword %1, off\n\tglobal_load_lds_dword %0, off\n\ts_waitcnt vmcnt(12)"
                 :: "v"(t1), "v"(t2), "s"(lbase + X_DUM) : "memory");
  } else {
    u32x4 bv;
    asm volatile("global_load_dwordx4 %0, %1, %2\n\ts_barrier" : "=&v"(bv) : "v"((lane & 7) * 16), "s"(a.b1 + fT * 64) : "memory");
    {
      unsigned vo0[4];
      w_offsets<F_ * 2>(lane, vo0);
      w_issue<F_ * 2, 0>((const char*)a.w1 + (size_t)(w * 32) * (F_ * 2) + fT * 128, vo0, lbase + w * (WL * 4096), 0, E0);
    }
    asm volatile("s_waitcnt vmcnt(%1)" : "+v"(bv) : "i"(4 * E0) : "memory");
    if (lane < 8) *(u32x4*)(lds + X_B1 + w * 128 + lane * 16) = bv;
  }
  asm volatile("" : "+v"(xr), "+v"(ep));
  const unsigned tag = __builtin_amdgcn_readfirstlane(ep) + 1u;
  STW(0, 1, 0);
  constexpr int RS = 1024 + 32;
  {
    float v[8];
#pragma unroll
    for (int j = 0; j < 4; ++j) { v[2 * j] = bf_lo(xr[j]); v[2 * j + 1] = bf_hi(xr[j]); }
    u32x4 o;
    ln8<true>(v, o);
    *(u32x4*)(lds + X_KT + w * RS + lane * 16) = o;
    ln8<false>(v, o);
    *(u32x4*)(lds + X_KT + (8 + w) * RS + lane * 16) = o;
  }
  if (w >= 4) asm volatile("s_waitcnt vmcnt(2)" ::: "memory");
  lds_barrier();
  STW(0, 2, 0);

  if (__builtin_expect(w < 4, 1)) {
    __builtin_amdgcn_s_setprio(3);
    const int g = lane >> 4, c = lane & 15, q = (lane & 15) >> 2, p = lane & 3;
    {
      u32x4 ov[2][2];
#pragma unroll
      for (int arr = 1; arr >= 0; --arr)
#pragma unroll
        for (int it = 0; it < 2; ++it) {
          const char* base = lds + X_KT + (arr * 8 + q) * RS + (w * 8 + it * 4 + g) * 32 + p * 8;
          s16x4 lo = __builtin_amdgcn_ds_read_tr16_b64_v4i16((LDSP s16x4*)(base));
          s16x4 hi = __builtin_amdgcn_ds_read_tr16_b64_v4i16((LDSP s16x4*)(base + 4 * RS));
          ov[arr][it] = __builtin_bit_cast(u32x4, __builtin_shufflevector(lo, hi, 0, 1, 2, 3, 4, 5, 6, 7));
        }
#pragma unroll
      for (int arr = 1; arr >= 0; --arr)
#pragma unroll
        for (int it = 0; it < 2; ++it) {
          const int dt = w * 8 + it * 4 + g;
          if (arr) st16_dev(a.Hf + ((size_t)((dt >> 2) * 64 + kpos(tb >> 2)) * 4 + (dt & 3)) * 1024 + ((tb & 3) * 16 + c) * 16, ov[arr][it]);
          else st16_dev(a.X2 + ((size_t)tb * 512 + dt * 16 + c) * 16, ov[arr][it]);
        }
    }
    const unsigned lring = lbase + w * (WL * 4096);
    TrAddr tra;
    tr_addr(tra, lds + w * (WL * 4096), lane);
    f32x4 acc[4][4], res[4];
    unsigned vo[4];
    w_offsets<F_ * 2>(lane, vo);
    const char* wb1 = (const char*)a.w1 + (size_t)(w * 32) * (F_ * 2) + fT * 128;
    const char* wb2 = (const char*)a.w2 + (size_t)(w * 32) * (L_ * 2) + sTl * 128;
    unsigned long long tH = 0;
    bf16x8 wr2[RW2][4];
    asm volatile("s_waitcnt vmcnt(0)" ::: "memory");
    grp_arrive(sy + 12, w, 1, lane);
    if (w == 0) {
      grp_wait<SPF>(sy + 12, 1);
      if (lane == 0) { st4_dev(a.fl + FL_H + tb * HSP, tag); sy[8] = 1; }
    } else word_wait<SPF>(sy + 8, 1);
    __builtin_amdgcn_s_setprio(0);
    STW(1, 0, 0);
    if (has1) {
      bf16x8 wr1[RW1][4];
      w_issue<F_ * 2, 0>(wb1, vo, lring, E0, WL);
      asm volatile("s_waitcnt vmcnt(%0)" :: "i"(4 * (WL - RW1)) : "memory");
      w_to_regs<RW1, 0>(tra, wr1);
      w_issue<F_ * 2, 0>(wb1, vo, lring, WL, WL + RW1);
      STW(1, 1, 0);
      if (w == 0 && !XPOLL) {
        poll_flags4(a.fl + FL_H + lane * 4 * HSP, tag);
        if (lane == 0) sy[11] = 1;
      } else word_wait<1>(sy + 11, 1);
      __builtin_amdgcn_s_setprio(3);
      STW(1, 2, 0);
      gemm_main<16, F_ * 2, false, RW1, 0>(a.Hf + (size_t)(dT * 64 + w) * 4096, a.Hf + (size_t)(dT * 64 + (w ^ 2)) * 4096, wb1, vo, lring, tra, wr1, w, lane, acc, lds + w * (WL * 4096) + lane * 16, sy + 19
      );
      STW(1, 3, 0);
      w_offsets<L_ * 2>(lane, vo);
      reduce4(acc, res, lds, w, lane, sy, 1);
      STW(1, 4, 0);
      u32x2 o[4];
#pragma unroll
      for (int j = 0; j < 4; ++j) {
        const u32x2 bb = *(const u32x2*)(lds + X_B1 + w * 128 + (16 * j + 4 * g) * 2);
        const float v0 = gelu_tanh(res[j][0] + bf_lo(bb[0])), v1 = gelu_tanh(res[j][1] + bf_hi(bb[0]));
        const float v2 = gelu_tanh(res[j][2] + bf_lo(bb[1])), v3 = gelu_tanh(res[j][3] + bf_hi(bb[1]));
        o[j] = u32x2{pk_bf16(v0, v1), pk_bf16(v2, v3)};
      }
#pragma unroll
      for (int jp = 0; jp < 2; ++jp) {
        u32x4 q4;
#pragma unroll
        for (int e = 0; e < 2; ++e) {
          auto sw = __builtin_amdgcn_permlane16_swap(o[2 * jp][e], o[2 * jp + 1][e], false, false);
          q4[e] = sw[0]; q4[2 + e] = sw[1];
        }
        const int f = fT * 64 + 16 * (2 * jp + (g & 1)) + 8 * (g >> 1);
        st16_dev(a.Gf + ((size_t)(dT * 56 + kpos(f >> 5)) * 4 + w) * 1024 + (((f & 31) >> 3) * 16 + c) * 16, q4);
      }
      grp_arrive(sy + 24, w, 1, lane);
      asm volatile("s_waitcnt vmcnt(0)" ::: "memory");
      grp_arrive(sy + 12, w, 2, lane);
      if (w == 0) {
        grp_wait<SPF>(sy + 12, 2);
        if (lane == 0) { st4_dev(a.fl + FL_G + (dT * 32 + fT) * GSP, tag); sy[9] = 1; }
      } else word_wait<SPF>(sy + 9, 1);
      STW(1, 5, 0);
      w_issue<L_ * 2, 3>(wb2, vo, lring, RW2, WL);
      word_wait<SPF>(sy + 28 + w, 1);
      w_to_regs<RW2, 3>(tra, wr2);
      __builtin_amdgcn_s_setprio(0);
    } else {
      const int bq = r_ - 28;
#pragma unroll 1
      for (int li = tid; li < 1792; li += 256) {
        if (li - tid == PFA * 256) {
          asm volatile("s_waitcnt vmcnt(0)" ::: "memory");
          if (w == 0) {
            __builtin_amdgcn_s_sleep(HDEL);
            poll_flags4(a.fl + FL_H + lane * 4 * HSP, tag);
            if (lane == 0) sy[11] = 1;
            tH = __builtin_amdgcn_s_memrealtime();
          }
          grp_wait<2>(sy + 4, 2);
        }
        touch(lbase + X_DUM, (const char*)a.w2 + (size_t)((li >> 2) * 4 + bq) * (L_ * 2) + (xg * 4 + (li & 3)) * 128);
        asm volatile("s_waitcnt vmcnt(3)" ::: "memory");
      }
      w_offsets<L_ * 2>(lane, vo);
      w_issue<L_ * 2, 3>(wb2, vo, lring, 0, WL);
      asm volatile("s_waitcnt vmcnt(0)" ::: "memory");
      w_to_regs<RW2, 3>(tra, wr2);
      STW(1, 6, 0);
    }
    static_assert(RW2 <= WL - 3, "");
    w_issue<L_ * 2, 3>(wb2, vo, lring, WL, RW2 + WL);
    STW(2, 0, 0);
    if (w == 0) {
      if (!has1) {
#pragma unroll 1
        for (int sp = 0; sp < 4096 && __builtin_amdgcn_s_memrealtime() - tH < IDEL; ++sp) __builtin_amdgcn_s_sleep(4);
      }
      poll_flags<PBK>(a.fl + FL_G + (dT2 * 32 + (lane & 31)) * GSP, tag, lane >= 28);
      if (lane == 0) sy[11] = 2;
    } else word_wait<1>(sy + 11, 2);
    STW(2, 1, 0);
    __builtin_amdgcn_s_setprio(3);
    gemm_main<14, L_ * 2, true, RW2, 3>(a.Gf + (size_t)(dT2 * 56 + w) * 4096, a.Gf + (size_t)(dT2 * 56 + (w ^ 2)) * 4096, wb2, vo, lring, tra, wr2, w, lane, acc, lds + w * (WL * 4096) + lane * 16, sy + 19
      );
    STW(2, 2, 0);
    u32x2 xv[4];
    unsigned braw[4];
    const size_t oo = (size_t)(sTl * 64 + c) * D_ + dT2 * 64 + 16 * w + 4 * g;
#pragma unroll
    for (int j = 0; j < 4; ++j) {
      braw[j] = a.b2[sTl * 64 + 16 * j + c];
      xv[j] = *(const u32x2*)((cus)a.x + oo + (size_t)(16 * j) * D_);
    }
    reduce4(acc, res, lds, w, lane, sy, 2);
    STW(2, 3, 0);
    word_wait<SPF>(sy + 10, 1);
    STW(2, 4, 0);
#pragma unroll
    for (int j = 0; j < 4; ++j) {
      const char* ap = lds + X_SC + (16 * w + 4 * g + q) * 144 + j * 32 + p * 8;
      const u32x2 sv = __builtin_bit_cast(u32x2, __builtin_amdgcn_ds_read_tr16_b64_v4i16((LDSP s16x4*)ap));
      const float bias = bf_lo(braw[j]);
      const float o0 = bf_lo(xv[j][0]) + bf_lo(sv[0]) * silu(res[j][0] + bias);
      const float o1 = bf_hi(xv[j][0]) + bf_hi(sv[0]) * silu(res[j][1] + bias);
      const float o2 = bf_lo(xv[j][1]) + bf_lo(sv[1]) * silu(res[j][2] + bias);
      const float o3 = bf_hi(xv[j][1]) + bf_hi(sv[1]) * silu(res[j][3] + bias);
      u32x2 o = {pk_bf16(o0, o1), pk_bf16(o2, o3)};
      *(u32x2*)(a.out + oo + (size_t)(16 * j) * D_) = o;
    }
    STW(2, 5, 0);
    STDUMP(0);
  } else {
    const int j4 = w - 4;
    volatile LDSP unsigned* xb = sy + 4;
    asm volatile("s_waitcnt vmcnt(2)" : "+v"(praw[0]), "+v"(praw[1]), "+v"(praw[2]), "+v"(praw[3]), "+v"(praw[4]), "+v"(praw[5]), "+v"(praw[6]), "+v"(praw[7]) :: "memory");
    const int ch = j4 >> 1, hw = j4 & 1;
    const int d = tb * 2 + ch;
    {
      float* par = (float*)(lds + X_SC + j4 * 2048);
      float* tabf = (float*)(lds + X_SC + 8192 + ch * (TABN * 8));
      const float dt = rbf(__expf(bf_lo(praw[6])));
      const float zr = dt * bf_lo(praw[0]) * LOG2E, zi = dt * bf_lo(praw[1]) * 0.15915494309189535f;
      const float br = dt * bf_lo(praw[2]), bi = dt * bf_lo(praw[3]), cr = bf_lo(praw[4]), ci = bf_lo(praw[5]);
      float wr = cr * br - ci * bi, wi = cr * bi + ci * br;
      const float zmax = wave_max_nn(fmaxf(zr, 0.f));
      const float wmax = wave_max_nn(fmaxf(fabsf(wr), fabsf(wi)));
      const unsigned we = (__float_as_uint(wmax) >> 23) & 0xff;
      const unsigned wec = we < 16 ? 16 : (we > 250 ? 250 : we);
      const float wsc = __uint_as_float((266u - wec) << 23);
      const float osc = __uint_as_float((wec - 12u) << 23) * 6.103515625e-05f;
      const float zs = zr - zmax;
      wr *= wsc; wi *= wsc;
      float qr, qi, er, ei;
      cexp2(zs * 128.f, zi * 128.f, qr, qi);
      cexp2(zs * 1024.f, zi * 1024.f, er, ei);
      *(f32x4*)(par + lane * 8) = f32x4{zs, zi, wr, wi};
      *(f32x4*)(par + lane * 8 + 4) = f32x4{qr, qi, er, ei};
      const float p2r = er * er - ei * ei, p2i = 2.f * er * ei;
      const float k2048 = wave_sum(wr * p2r - wi * p2i) * 16384.f;
      STW(3, 0, 4);
      if (TSPL == 1 && j4 < 2) {
        word_wait<1>(sy + 8, 1);
        const int l1 = 4096 + r_ * 128 + j4 * 64 + lane;
        touch(lbase + X_DUM, (const char*)a.w1 + (size_t)(l1 >> 2) * (F_ * 2) + (((xg * 28) >> 3) + (l1 & 3)) * 128);
      }
      const int r = lane & 15, g = lane >> 4;
      const float va = 8.f * r;
      const int off = r < 8 ? r : r < 12 ? 256 + 2 * (r - 8) : 512 + 256 * ((r - 12) >> 1) + 4 * (r & 1);
      const float vb = (float)off;
      f32x4 acc[2][2];
#pragma unroll
      for (int t = 0; t < 4; ++t) acc[t >> 1][t & 1] = f32x4{0.f, 0.f, 0.f, 0.f};
#pragma unroll 1
      for (int ks = 0; ks < 4; ++ks) {
        u32x4 a0, a1, b0, b1;
#pragma unroll
        for (int j = 0; j < 4; ++j) {
          const int n = ks * 16 + g * 4 + j;
          const f32x4 pp = *(const f32x4*)(par + n * 8), pq = *(const f32x4*)(par + n * 8 + 4);
          float tr, ti, xr_, xi_;
          cexp2(__builtin_fmaf(pp[0], va, 14.f), pp[1] * va, tr, ti);
          cexp2(pp[0] * vb, pp[1] * vb, xr_, xi_);
          const float ur = pp[2] * xr_ - pp[3] * xi_, ui = pp[2] * xi_ + pp[3] * xr_;
          a0[j] = pk_f16(tr, ti);
          a1[j] = pk_f16(tr * pq[0] - ti * pq[1], tr * pq[1] + ti * pq[0]);
          b0[j] = pk_f16(ur, -ui);
          b1[j] = pk_f16(ur * pq[2] - ui * pq[3], -(ur * pq[3] + ui * pq[2]));
        }
        acc[0][0] = __builtin_amdgcn_mfma_f32_16x16x32_f16(__builtin_bit_cast(f16x8, a0), __builtin_bit_cast(f16x8, b0), acc[0][0], 0, 0, 0);
        acc[1][0] = __builtin_amdgcn_mfma_f32_16x16x32_f16(__builtin_bit_cast(f16x8, a1), __builtin_bit_cast(f16x8, b0), acc[1][0], 0, 0, 0);
        acc[0][1] = __builtin_amdgcn_mfma_f32_16x16x32_f16(__builtin_bit_cast(f16x8, a0), __builtin_bit_cast(f16x8, b1), acc[0][1], 0, 0, 0);
        acc[1][1] = __builtin_amdgcn_mfma_f32_16x16x32_f16(__builtin_bit_cast(f16x8, a1), __builtin_bit_cast(f16x8, b1), acc[1][1], 0, 0, 0);
      }
      STW(3, 1, 4);
      if (XPOLL && has1 && j4 == 0) {
        poll_flags4(a.fl + FL_H + lane * 4 * HSP, tag);
        if (lane == 0) sy[11] = 1;
      }
      float kl = k2048 * osc;
#pragma unroll
      for (int t = 0; t < 4; ++t) acc[t >> 1][t & 1] *= osc;
      if (zmax > 0.f) {
#pragma unroll
        for (int t = 0; t < 4; ++t)
#pragma unroll
          for (int e = 0; e < 4; ++e)
            acc[t >> 1][t & 1][e] *= __builtin_amdgcn_exp2f(zmax * (vb + (float)(8 * (16 * (t >> 1) + e) + 1024 * (t & 1)) + 32.f * g));
        kl *= __builtin_amdgcn_exp2f(zmax * 2048.f);
      }
      {
        const int stride = r < 8 ? 8 : r < 12 ? 4 : 2;
        const int base = r < 8 ? r : r < 12 ? 256 + (r - 8) : 384 + 64 * ((r - 12) >> 1) + (r & 1);
        float* p0 = tabf + (base + stride * 4 * g) * 2 + hw;
#pragma unroll
        for (int it = 0; it < 2; ++it)
#pragma unroll
          for (int e = 0; e < 4; ++e) p0[stride * (16 * it + e) * 2] = acc[it][0][e];
        if ((off & 255) == 0) {
          float* p1 = tabf + (512 + (off >> 3) + 4 * g) * 2 + hw;
#pragma unroll
          for (int it = 0; it < 2; ++it)
#pragma unroll
            for (int e = 0; e < 4; ++e) p1[(16 * it + e) * 2] = acc[it][1][e];
        }
        if (lane < 2) tabf[(640 + lane) * 2 + hw] = lane ? 0.f : kl;
      }
      grp_arrive(xb, j4, 1, lane);
      const int l2 = hw * 64 + lane;
      char* lk = lds + X_KT + ch * KT_B;
      int ix[17];
#pragma unroll
      for (int e = 0; e < 17; e += 2) {
        const int j0 = 2047 - 16 * l2 - e, j1 = j0 - 1;
        const unsigned bb = pk_bf16((float)j0, (float)j1);
        const int i0 = (int)(bb & 0xffff) - (0x4300 - 128), i1 = (int)(bb >> 16) - (0x4300 - 128);
        ix[e] = j0 < 128 ? (j0 < 0 ? 641 : j0) : i0;
        if (e + 1 < 17) ix[e + 1] = j1 < 128 ? j1 : i1;
      }
      word_wait<1>(sy + 8, 1);
      if (l2 < 8) {
        const int pc = (l2 & 2) ? 258 + (l2 & 1) : (l2 & 1);
        *(u32x4*)(lk + (l2 >> 2) * R1OFF + pc * 16) = u32x4{0, 0, 0, 0};
      }
      if (j4 == 0) *(u32x4*)(lds + X_U + (lane & 1) * U_B + (lane >> 1) * 16) = u32x4{0, 0, 0, 0};
#pragma unroll
      for (int e = 0; e < 17; ++e) asm volatile("" : "+v"(ix[e]));
      grp_wait<0>(xb, 1);
      f32x2 tv[17];
#pragma unroll
      for (int e = 0; e < 17; ++e) tv[e] = *(const f32x2*)(tabf + ix[e] * 2);
      __builtin_amdgcn_sched_barrier(0);
      float val[17];
#pragma unroll
      for (int e = 0; e < 17; ++e) val[e] = tv[e][0] + tv[e][1];
#pragma unroll
      for (int hf = 0; hf < 2; ++hf) {
        u32x4 o0, o1;
#pragma unroll
        for (int e = 0; e < 4; ++e) { o0[e] = pk_bf16(val[8 * hf + 2 * e], val[8 * hf + 2 * e + 1]); o1[e] = pk_bf16(val[8 * hf + 2 * e + 1], val[8 * hf + 2 * e + 2]); }
        *(u32x4*)(lk + 32 + l2 * 32 + hf * 16) = o0;
        *(u32x4*)(lk + R1OFF + 32 + l2 * 32 + hf * 16) = o1;
      }
      if (l2 == 0) *(unsigned*)(lk + R1OFF + 28) = pk_bf16(0.f, val[0]);
      STW(3, 2, 4);
    }
    word_wait(sy + 11, 1);
    unsigned vo2[4];
    w_offsets<L_ * 2>(lane, vo2);
    if (GLATE && has1) word_wait<1>(sy + j4, 1);
    STW(3, 3, 4);
    {
      u32x4 u0, u1;
      const int t0 = j4 * 64 + (lane >> 1);
      asm volatile("global_load_dwordx4 %0, %2, off sc1\n\tglobal_load_dwordx4 %1, %3, off sc1" : "=&v"(u0), "=&v"(u1)
                   : "v"(a.X2 + ((size_t)t0 * 512 + tb * 2 + (lane & 1)) * 16), "v"(a.X2 + ((size_t)(t0 + 32) * 512 + tb * 2 + (lane & 1)) * 16) : "memory");
      if (GLATE && has1) {
        w_issue<L_ * 2, 3>((const char*)a.w2 + (size_t)(j4 * 32) * (L_ * 2) + sTl * 128, vo2, lbase + j4 * (WL * 4096), 0, RW2);
        asm volatile("s_waitcnt vmcnt(%0)" :: "i"(4 * RW2) : "memory");
      } else asm volatile("s_waitcnt vmcnt(0)" ::: "memory");
      asm volatile("" : "+v"(u0), "+v"(u1));
      char* lu = lds + X_U + (lane & 1) * U_B;
      *(u32x4*)(lu + UPAD * 2 + t0 * 16) = u0;
      *(u32x4*)(lu + UPAD * 2 + (t0 + 32) * 16) = u1;
    }
    grp_arrive(xb, j4, 2, lane);
    if (GLATE && has1) {
      asm volatile("s_waitcnt vmcnt(0)" ::: "memory");
      if (lane == 0) sy[28 + j4] = 1;
    }
    grp_wait<0>(xb, 2);
    if (b == 0 && tid == 256) st4_dev(a.fl, tag);
    STW(3, 8, 4);
    {
      const float dsk = bf_lo(praw[7]);
      const char* lk = lds + X_KT + ch * KT_B;
      const char* lu = lds + X_U + ch * U_B;
      const int col = lane & 15, g = lane >> 4;
      const int p00 = 2047 - col + 8 * g;
      const char* gbase = lk + ((p00 & 1) ? R1OFF : 0) + (p00 >> 1) * 4 - 256 * hw;
      const char* bbase = lu + (UPAD - 16 + 16 * col + 8 * g) * 2 - 256 * hw;
      f32x4 acc[8];
#pragma unroll
      for (int i = 0; i < 8; ++i) acc[i] = f32x4{0.f, 0.f, 0.f, 0.f};
      bf16x8 bf[2][8], gf[2][8];
#define CLD(set, rr) do { _Pragma("unroll") for (int i = 0; i < 8; ++i) { \
          bf[set][i] = *(const bf16x8*)(bbase + 64 * (8 * i - (rr))); \
          const unsigned* gp = (const unsigned*)(gbase - 64 * (8 * i + (rr))); \
          u32x4 gv = {gp[0], gp[1], gp[2], gp[3]}; gf[set][i] = __builtin_bit_cast(bf16x8, gv); } } while (0)
      CLD(0, 0);
      CLD(1, 1);
      if (has1 && !GLATE) {
        word_wait<1>(sy + j4, 1);
        w_issue<L_ * 2, 3>((const char*)a.w2 + (size_t)(j4 * 32) * (L_ * 2) + sTl * 128, vo2, lbase + j4 * (WL * 4096), 0, RW2);
        asm volatile("s_waitcnt vmcnt(0)" ::: "memory");
        if (lane == 0) sy[28 + j4] = 1;
      }
      if (CGATE == 1 && has1) word_wait<2>(sy + 9, 1);
      if (CGATE == 2 && has1) grp_wait<1>(sy + 24, 1);
      STW(3, 4, 4);
#pragma unroll
      for (int rr = 0; rr < 4; ++rr) {
#pragma unroll
        for (int bq = 0; bq < 8; ++bq)
#pragma unroll
          for (int i = 0; i + bq < 8; ++i) acc[i + bq] = __builtin_amdgcn_mfma_f32_16x16x32_bf16(gf[rr & 1][bq], bf[rr & 1][i], acc[i + bq], 0, 0, 0);
        asm volatile("" : "+v"(acc[0]), "+v"(acc[1]), "+v"(acc[2]), "+v"(acc[3]), "+v"(acc[4]), "+v"(acc[5]), "+v"(acc[6]), "+v"(acc[7]) :: "memory");
        if (rr + 2 < 4) CLD(rr & 1, rr + 2);
        __builtin_amdgcn_sched_barrier(0);
      }
#undef CLD
      __builtin_amdgcn_s_setprio(XPR);
      STW(3, 5, 4);
      float* xc = (float*)(lds + X_SC + ch * 8192);
      f32x4 keep[4];
#pragma unroll
      for (int i = 0; i < 4; ++i) {
        f32x4 give;
#pragma unroll
        for (int e = 0; e < 4; ++e) { give[e] = hw ? acc[2 * i][e] : acc[2 * i + 1][e]; keep[i][e] = hw ? acc[2 * i + 1][e] : acc[2 * i][e]; }
        *(f32x4*)(xc + ((hw * 4 + i) * 64 + lane) * 4) = give;
      }
      grp_bar<0>(xb, j4, 3, lane);
      u32x2 o[4];
#pragma unroll
      for (int i = 0; i < 4; ++i) {
        const f32x4 y = keep[i] + *(const f32x4*)(xc + (((hw ^ 1) * 4 + i) * 64 + lane) * 4);
        const int t = 256 * (2 * i + hw) + 16 * col + 4 * g;
        const u32x2 uu = *(const u32x2*)(lu + (UPAD + t) * 2);
        o[i] = u32x2{pk_bf16(y[0] + bf_lo(uu[0]) * dsk, y[1] + bf_hi(uu[0]) * dsk), pk_bf16(y[2] + bf_lo(uu[1]) * dsk, y[3] + bf_hi(uu[1]) * dsk)};
      }
#pragma unroll
      for (int ip = 0; ip < 2; ++ip) {
        u32x4 q4;
#pragma unroll
        for (int e = 0; e < 2; ++e) {
          auto sw = __builtin_amdgcn_permlane16_swap(o[2 * ip][e], o[2 * ip + 1][e], false, false);
          q4[e] = sw[0]; q4[2 + e] = sw[1];
        }
#pragma unroll
        for (int e = 0; e < 4; ++e) q4[e] = min(q4[e], 0xfffffffeu);
        const int t = 256 * (2 * (2 * ip + (g & 1)) + hw) + 16 * col + 8 * (g >> 1);
        st16_dev(a.sT + ((size_t)d * L_ + t) * 2, q4);
      }
      grp_arrive(xb, j4, 4, lane);
      STW(3, 6, 4);
    }
    {
      __builtin_amdgcn_s_setprio(0);
      word_wait<2>(sy + 19, 1);
      grp_wait<1>(xb, 4);
      __builtin_amdgcn_s_setprio(XPR);
      u32x4 s0, s1;
      const int rw_ = j4 * 16 + (lane >> 3);
      const char* sp = a.sT + ((size_t)(dT2 * 64 + rw_) * L_ + sTl * 64) * 2 + (lane & 7) * 16;
#pragma unroll 1
      for (int att = 0; att < 4096; ++att) {
        asm volatile("global_load_dwordx4 %0, %2, off sc1\n\tglobal_load_dwordx4 %1, %3, off sc1\n\ts_waitcnt vmcnt(0)" : "=&v"(s0), "=&v"(s1)
                     : "v"(sp), "v"(sp + (size_t)8 * L_ * 2) : "memory");
        const unsigned mx = max(max(max(s0[0], s0[1]), max(s0[2], s0[3])), max(max(s1[0], s1[1]), max(s1[2], s1[3])));
        if (!__any(mx == ~0u)) break;
        __builtin_amdgcn_s_sleep(SBK);
      }
      *(u32x4*)(lds + X_SC + rw_ * 144 + (lane & 7) * 16) = s0;
      *(u32x4*)(lds + X_SC + (rw_ + 8) * 144 + (lane & 7) * 16) = s1;
      grp_bar(xb, j4, 5, lane);
      if (tid == 256) sy[10] = 1;
      STW(3, 7, 4);
      STDUMP(4);
    }
  }
}

__device__ __attribute__((noinline)) void code_end_marker() { asm volatile("s_nop 0"); }

extern "C" void kernel_launch(void* const* d_in, const int* in_sizes, int n_in, void* d_out, int out_size, void* d_ws,
                              size_t ws_size, hipStream_t stream) {
  char* ws = (char*)d_ws;
  Args a;
  a.x = (const u32x4*)d_in[0];
  a.Are = (cus)d_in[1]; a.Aim = (cus)d_in[2]; a.Bre = (cus)d_in[3]; a.Bim = (cus)d_in[4]; a.Cre = (cus)d_in[5]; a.Cim = (cus)d_in[6];
  a.logdt = (cus)d_in[7]; a.Dskip = (cus)d_in[8];
  a.w1 = (cus)d_in[10]; a.b1 = (cus)d_in[11]; a.w2 = (cus)d_in[12]; a.b2 = (cus)d_in[13];
  a.out = (unsigned short*)d_out;
  a.X2 = ws; a.Hf = ws + (2u << 20); a.Gf = ws + (4u << 20); a.sT = ws + (6u << 20); a.fl = (unsigned*)(ws + (8u << 20));
  (void)hipFuncSetAttribute((const void*)k_mega, hipFuncAttributeMaxDynamicSharedMemorySize, LDS_B);
  k_mega<<<256, 512, LDS_B, stream>>>(a STPASS);
}
```

```cpp
#include <hip/hip_runtime.h>
#include <hip/hip_bf16.h>
#include <stdint.h>

typedef short s16x4 __attribute__((ext_vector_type(4)));
typedef short bf16x8 __attribute__((ext_vector_type(8)));
typedef _Float16 f16x8 __attribute__((ext_vector_type(8)));
typedef _Float16 h2_t __attribute__((ext_vector_type(2)));
typedef __bf16 bf2_t __attribute__((ext_vector_type(2)));
typedef float f32x2 __attribute__((ext_vector_type(2)));
typedef float f32x4 __attribute__((ext_vector_type(4)));
typedef unsigned u32x2 __attribute__((ext_vector_type(2)));
typedef unsigned u32x4 __attribute__((ext_vector_type(4)));
typedef const unsigned short* cus;
#define DI __device__ __forceinline__
#define LDSP __attribute__((address_space(3)))

#define STW(k, slot, wv)
#define STDECL
#define STDUMP(wv)
#define STARG
#define STPASS
#define RW1 5
#define RW2 4
#define WL 7
#define AD 3
#define PBK 6
#define CGATE 2
#define IDEL 550
#define SDEL 32
#define SBK 8
#define HSP 1
#define GSP 32
#define HDEL 8
#define PSTEP 6
#define XPR 3
#define PFA 3
#define E1 0
#define E2 0
#define SPF 0
#define APOL " sc1"
constexpr int L_ = 2048, D_ = 512, N_ = 128, F_ = 1792;
constexpr int TABN = 648;
constexpr float LOG2E = 1.44269504088896341f;
constexpr int SPIN_CAP = 1 << 18;

DI unsigned pk_bf16(float lo, float hi) {
  f32x2 f = {lo, hi};
  return __builtin_bit_cast(unsigned, __builtin_convertvector(f, bf2_t));
}
DI unsigned pk_f16(float lo, float hi) {
  f32x2 f = {lo, hi};
  return __builtin_bit_cast(unsigned, __builtin_convertvector(f, h2_t));
}
DI float bf_lo(unsigned u) { return __uint_as_float(u << 16); }
DI float bf_hi(unsigned u) { return __uint_as_float(u & 0xffff0000u); }
DI float rbf(float x) { return bf_lo(pk_bf16(x, 0.f)); }
DI float bf2f(unsigned short u) { return __uint_as_float(((unsigned)u) << 16); }

template <int CTRL, int RM, bool BC> DI float dppf(float v) {
  return __builtin_bit_cast(float, __builtin_amdgcn_update_dpp(0, __builtin_bit_cast(int, v), CTRL, RM, 0xf, BC));
}
DI float wave_sum(float v) {
  v += dppf<0x111, 0xf, true>(v);
  v += dppf<0x112, 0xf, true>(v);
  v += dppf<0x114, 0xf, true>(v);
  v += dppf<0x118, 0xf, true>(v);
  v += dppf<0x142, 0xa, false>(v);
  v += dppf<0x143, 0xc, false>(v);
  return __builtin_bit_cast(float, __builtin_amdgcn_readlane(__builtin_bit_cast(int, v), 63));
}
DI float wave_max_nn(float v) {
  v = fmaxf(v, dppf<0x111, 0xf, true>(v));
  v = fmaxf(v, dppf<0x112, 0xf, true>(v));
  v = fmaxf(v, dppf<0x114, 0xf, true>(v));
  v = fmaxf(v, dppf<0x118, 0xf, true>(v));
  v = fmaxf(v, dppf<0x142, 0xa, false>(v));
  v = fmaxf(v, dppf<0x143, 0xc, false>(v));
  return __builtin_bit_cast(float, __builtin_amdgcn_readlane(__builtin_bit_cast(int, v), 63));
}
DI void touch(unsigned ldum, const void* p) { asm volatile("s_mov_b32 m0, %0\n\ts_nop 0\n\tglobal_load_lds_dword %1, off" :: "s"(ldum), "v"(p) : "memory"); }
DI void lds_barrier() { asm volatile("s_waitcnt lgkmcnt(0)" ::: "memory"); __builtin_amdgcn_s_barrier(); asm volatile("" ::: "memory"); }
DI void st16_dev(void* p, u32x4 v) { asm volatile("global_store_dwordx4 %0, %1, off sc1\n\ts_nop 1" :: "v"(p), "v"(v) : "memory"); }
DI void st8_dev(void* p, u32x2 v) { asm volatile("global_store_dwordx2 %0, %1, off sc1" :: "v"(p), "v"(v) : "memory"); }
DI void st4_dev(void* p, unsigned v) { asm volatile("global_store_dword %0, %1, off sc1" :: "v"(p), "v"(v) : "memory"); }
DI unsigned ld4_dev_wait(const void* p) { unsigned r; asm volatile("global_load_dword %0, %1, off sc1\n\ts_waitcnt vmcnt(0)" : "=&v"(r) : "v"(p) : "memory"); return r; }

DI void grp_arrive(volatile LDSP unsigned* words, int idx, unsigned ph, int lane) {
  asm volatile("s_waitcnt lgkmcnt(0)" ::: "memory");
  if (lane == 0) words[idx] = ph;
}
template <int SL = 1>
DI void grp_wait(volatile LDSP unsigned* words, unsigned ph) {
#pragma unroll 1
  for (int sp = 0; sp < SPIN_CAP * 8; ++sp) {
    const u32x4 v = *(volatile LDSP u32x4*)words;
    const unsigned m = min(min(v[0], v[1]), min(v[2], v[3]));
    if (__builtin_amdgcn_readfirstlane(m) >= ph) break;
    if (SL) __builtin_amdgcn_s_sleep(SL);
  }
  asm volatile("" ::: "memory");
}
template <int SL = 1>
DI void grp_bar(volatile LDSP unsigned* words, int idx, unsigned ph, int lane) { grp_arrive(words, idx, ph, lane); grp_wait<SL>(words, ph); }
template <int SL = 1>
DI void word_wait(volatile LDSP unsigned* word, unsigned v) {
#pragma unroll 1
  for (int sp = 0; sp < SPIN_CAP * 8; ++sp) {
    if (__builtin_amdgcn_readfirstlane(*word) >= v) break;
    if (SL) __builtin_amdgcn_s_sleep(SL);
  }
  asm volatile("" ::: "memory");
}
template <int BK = PBK>
DI void poll_flags(const unsigned* p, unsigned tag, bool done) {
#pragma unroll 1
  for (int sp = 0; sp < SPIN_CAP; ++sp) {
    if (!done) done = ld4_dev_wait(p) == tag;
    if (__all(done)) break;
    __builtin_amdgcn_s_sleep(BK);
  }
}
DI void poll_flags4(const unsigned* p, unsigned tag) {
  bool done = false;
#pragma unroll 1
  for (int sp = 0; sp < SPIN_CAP; ++sp) {
    if (!done) {
      u32x4 f;
      asm volatile("global_load_dwordx4 %0, %1, off sc1\n\ts_waitcnt vmcnt(0)" : "=&v"(f) : "v"(p) : "memory");
      done = f[0] == tag && f[1] == tag && f[2] == tag && f[3] == tag;
    }
    if (__all(done)) break;
    __builtin_amdgcn_s_sleep(PBK);
  }
}

DI int kpos(int kb) { return kb ^ (((kb >> 2) & 1) << 1); }

template <bool PIVOT>
DI void ln8(float (&v)[8], u32x4& o) {
  const float p = PIVOT ? __builtin_bit_cast(float, __builtin_amdgcn_readfirstlane(__builtin_bit_cast(int, v[0]))) : 0.f;
  float s0 = 0.f, s1 = 0.f, q0 = 0.f, q1 = 0.f;
#pragma unroll
  for (int i = 0; i < 4; ++i) { const float t = v[i] - p, u = v[i + 4] - p; s0 += t; s1 += u; q0 = __builtin_fmaf(t, t, q0); q1 = __builtin_fmaf(u, u, q1); }
  const float ds = wave_sum(s0 + s1) * (1.f / D_), dq = wave_sum(q0 + q1) * (1.f / D_);
  const float mean = p + ds, var = fmaxf(__builtin_fmaf(-ds, ds, dq), 0.f);
  const float mb = rbf(mean), vb = rbf(var);
  const float den = rbf(__builtin_amdgcn_sqrtf(rbf(vb + 1e-5f)));
  const float inv = __builtin_amdgcn_rcpf(den);
#pragma unroll
  for (int i = 0; i < 4; ++i) {
    const unsigned t = pk_bf16(v[2 * i] - mb, v[2 * i + 1] - mb);
    o[i] = pk_bf16(bf_lo(t) * inv, bf_hi(t) * inv);
    v[2 * i] = bf_lo(o[i]); v[2 * i + 1] = bf_hi(o[i]);
  }
}
DI void cexp2(float le, float ph, float& re, float& im) {
  const float e = __builtin_amdgcn_exp2f(le);
  const float p = __builtin_amdgcn_fractf(ph);
  re = e * __builtin_amdgcn_cosf(p);
  im = e * __builtin_amdgcn_sinf(p);
}
DI float gelu_tanh(float x) {
  const float u = x * (1.f + 0.044715f * x * x);
  return x * __builtin_amdgcn_rcpf(1.f + __builtin_amdgcn_exp2f(-2.f * 0.7978845608028654f * LOG2E * u));
}
DI float silu(float x) { return x * __builtin_amdgcn_rcpf(1.f + __builtin_amdgcn_exp2f(-LOG2E * x)); }

#define DMA4(ldsaddr, vo, sb) do { unsigned keep_; \
    asm volatile("s_mov_b32 %0, m0\n\ts_mov_b32 m0, %1\n\ts_nop 0\n\t" \
                 "global_load_lds_dwordx4 %2, %6\n\tglobal_load_lds_dwordx4 %3, %6 offset:1024\n\t" \
                 "global_load_lds_dwordx4 %4, %6 offset:2048\n\tglobal_load_lds_dwordx4 %5, %6 offset:3072\n\ts_mov_b32 m0, %0" \
                 : "=&s"(keep_) : "s"(__builtin_amdgcn_readfirstlane(ldsaddr)), "v"(vo[0]), "v"(vo[1]), "v"(vo[2]), "v"(vo[3]), "s"(sb) : "memory"); } while (0)
template <int RB> DI void w_offsets(int lane, unsigned (&vo)[4]) {
  const int lr = lane >> 3, p8 = lane & 7;
#pragma unroll
  for (int j = 0; j < 4; ++j) vo[j] = (8 * j + lr) * RB + ((p8 ^ ((lr & 2) | ((j & 1) << 2))) << 4) - 1024 * j;
}
struct TrAddr { const char* p[4]; };
DI void tr_addr(TrAddr& t, const char* ring, int lane) {
  const int g = lane >> 4, q = (lane & 15) >> 2, p = lane & 3;
  const int frow = (q & 2) | ((g & 1) << 2);
  const char* trb = ring + (8 * g + q) * 128 + (p & 1) * 8;
#pragma unroll
  for (int j = 0; j < 4; ++j) t.p[j] = trb + (((2 * j + (p >> 1)) ^ frow) << 4);
}
DI void tr_read(bf16x8 (&fr)[4], const TrAddr& t, int off) {
#pragma unroll
  for (int j = 0; j < 4; ++j) {
    s16x4 lo = __builtin_amdgcn_ds_read_tr16_b64_v4i16((LDSP s16x4*)(t.p[j] + off));
    s16x4 hi = __builtin_amdgcn_ds_read_tr16_b64_v4i16((LDSP s16x4*)(t.p[j] + off + 512));
    fr[j] = __builtin_shufflevector(lo, hi, 0, 1, 2, 3, 4, 5, 6, 7);
  }
}
template <int RB, int OFF>
DI void w_issue(const char* wb, const unsigned (&vo)[4], unsigned lring, int k0, int k1) {
#pragma unroll 1
  for (int k = k0; k < k1; ++k) DMA4(lring + ((k + OFF) % WL) * 4096, vo, wb + (size_t)k * 128 * RB);
}
template <int RWN, int OFF>
DI void w_to_regs(const TrAddr& tra, bf16x8 (&wr)[RWN][4]) {
#pragma unroll
  for (int k = 0; k < RWN; ++k) tr_read(wr[k], tra, ((k + OFF) % WL) * 4096);
#pragma unroll
  for (int k = 0; k < RWN; ++k) asm volatile("s_waitcnt lgkmcnt(0)" : "+v"(wr[k][0]), "+v"(wr[k][1]), "+v"(wr[k][2]), "+v"(wr[k][3]) :: "memory");
}
template <int NKB, int RWN, int OFF> struct Sch {
  static constexpr int NL = NKB - RWN;
  static constexpr int blk(int i) { return i < NL ? RWN + i : i - NL; }
  static constexpr int slot(int i) { return (RWN + i + OFF) % WL; }
  static constexpr bool dma(int s) { return s >= 1 && s - 1 + WL < NL; }
  static constexpr int younger(int i) {
    int pos = 0, pa = -1, pw = -1;
    for (int k = 0; k < AD; ++k) { if (k == i) pa = pos; ++pos; }
    for (int s = 0; s < i; ++s) {
      if (dma(s)) { if (s - 1 + WL == i + 1) pw = pos; ++pos; }
      if (s + AD < NKB) { if (s + AD == i) pa = pos; ++pos; }
    }
    const int need = pa > pw ? pa : pw;
    return pos - 1 - need;
  }
};
DI bf16x8 tr_frag(const char* p) {
  s16x4 lo = __builtin_amdgcn_ds_read_tr16_b64_v4i16((LDSP s16x4*)(p));
  s16x4 hi = __builtin_amdgcn_ds_read_tr16_b64_v4i16((LDSP s16x4*)(p + 512));
  return __builtin_shufflevector(lo, hi, 0, 1, 2, 3, 4, 5, 6, 7);
}
template <int NKB, int RB, bool SWAP, int RWN, int OFF>
DI void gemm_main(const char* ab, const char* abo, const char* wb, const unsigned (&vo)[4], unsigned lring, const TrAddr& tra,
                  bf16x8 (&wr)[RWN][4], int w, int lane, f32x4 (&acc)[4][4], char* park, volatile LDSP unsigned* sig, unsigned long long* lst = nullptr) {
  typedef Sch<NKB, RWN, OFF> S;
  unsigned va[4];
#pragma unroll
  for (int t = 0; t < 4; ++t) va[t] = lane * 16 + ((t + w + 1) & 3) * 1024;
  u32x4 av[AD][4];
  bf16x8 fr[4];
#define ABASE(i) (((S::blk(i) & 1) ? abo : ab) + (size_t)S::blk(i) * 16384)
#define LDA1(i, t) asm volatile("global_load_dwordx4 %0, %1, %2" APOL : "=&v"(av[(i) % AD][t]) : "v"(va[t]), "s"(ABASE(i)) : "memory")
#define DMA1(i, q) asm volatile("s_mov_b32 m0, %0\n\ts_nop 0\n\tglobal_load_lds_dwordx4 %1, %2 offset:%3" \
                 :: "s"(__builtin_amdgcn_readfirstlane(lring + S::slot((i) - 1) * 4096)), "v"(vo[q]), "s"(wb + (size_t)(RWN + (i) - 1 + WL) * 128 * RB), "i"((q) * 1024) : "memory")
#pragma unroll
  for (int i = 0; i < AD; ++i) { LDA1(i, 0); LDA1(i, 1); LDA1(i, 2); LDA1(i, 3); }
  asm volatile("s_waitcnt vmcnt(%0)" :: "i"(4 * AD) : "memory");
#pragma unroll
  for (int j = 0; j < 4; ++j) fr[j] = tr_frag(tra.p[j] + S::slot(0) * 4096);
#pragma unroll
  for (int i = 0; i < NKB; ++i) {
    const int st = i % AD;
    asm volatile("s_waitcnt vmcnt(%4)" : "+v"(av[st][0]), "+v"(av[st][1]), "+v"(av[st][2]), "+v"(av[st][3]) : "i"(4 * S::younger(i)) : "memory");
    if (SWAP && i == PSTEP && lane == 0) *sig = 1;
    if (i == NKB - 1) {
#pragma unroll
      for (int m = 0; m < 16; ++m) {
        const int t = m >> 2, j = m & 3;
        if (SWAP) asm volatile("v_mfma_f32_16x16x32_bf16 %0, %1, %2, %0" : "+v"(acc[j][t]) : "v"(av[st][t]), "v"(wr[RWN - 1][j]) : "memory");
        else asm volatile("v_mfma_f32_16x16x32_bf16 %0, %1, %2, %0" : "+v"(acc[j][t]) : "v"(wr[RWN - 1][j]), "v"(av[st][t]) : "memory");
        if (m >= 8) *(f32x4*)(park + (m - 8) * 1024) = acc[(m - 8) & 3][(m - 8) >> 2];
      }
      continue;
    }
#pragma unroll
    for (int j = 0; j < 4; ++j) {
#pragma unroll
      for (int t = 0; t < 4; ++t) {
        const bf16x8 wf = i < S::NL ? fr[j] : wr[i < S::NL ? 0 : i - S::NL][j];
        if (i == 0) {
          if (SWAP) asm volatile("v_mfma_f32_16x16x32_bf16 %0, %1, %2, 0" : "=&v"(acc[j][t]) : "v"(av[st][t]), "v"(wf) : "memory");
          else asm volatile("v_mfma_f32_16x16x32_bf16 %0, %1, %2, 0" : "=&v"(acc[j][t]) : "v"(wf), "v"(av[st][t]) : "memory");
        } else {
          if (SWAP) asm volatile("v_mfma_f32_16x16x32_bf16 %0, %1, %2, %0" : "+v"(acc[j][t]) : "v"(av[st][t]), "v"(wf) : "memory");
          else asm volatile("v_mfma_f32_16x16x32_bf16 %0, %1, %2, %0" : "+v"(acc[j][t]) : "v"(wf), "v"(av[st][t]) : "memory");
        }
        if (S::dma(i) && j < 2 && (t == 0 || t == 2)) { if (j == 0 && t == 0) DMA1(i, 0); else if (j == 0) DMA1(i, 1); else if (t == 0) DMA1(i, 2); else DMA1(i, 3); }
        if (j == 3 && i + AD < NKB) { if (t == 0) LDA1(i + AD, 0); else if (t == 1) LDA1(i + AD, 1); else if (t == 2) LDA1(i + AD, 2); else LDA1(i + AD, 3); }
      }
      if (i + 1 < S::NL) fr[j] = tr_frag(tra.p[j] + S::slot(i + 1) * 4096);
    }
  }
#pragma unroll
  for (int j = 0; j < 4; ++j) { *(f32x4*)(park + (8 + j) * 1024) = acc[j][2]; asm volatile("" ::: "memory"); }
  asm volatile("s_nop 7\n\ts_nop 7" ::: "memory");
#undef LDA1
#undef DMA1
#undef ABASE
}
DI void reduce4(f32x4 (&acc)[4][4], f32x4 (&res)[4], char* lds, int w, int lane, volatile LDSP unsigned* words, unsigned ph) {
  grp_bar<SPF>(words, w, ph, lane);
  f32x4 pv[3][4];
#pragma unroll
  for (int k = 1; k < 4; ++k) {
    const char* p = lds + ((w + k) & 3) * (WL * 4096) + (3 - k) * 4096 + lane * 16;
#pragma unroll
    for (int j = 0; j < 4; ++j) pv[k - 1][j] = *(const f32x4*)(p + j * 1024);
  }
#pragma unroll
  for (int j = 0; j < 4; ++j) res[j] = (acc[j][3] + pv[0][j]) + (pv[1][j] + pv[2][j]);
}

struct Args {
  const u32x4* x; cus Are, Aim, Bre, Bim, Cre, Cim, logdt, Dskip, w1, b1, w2, b2;
  unsigned short* out;
  char* Hf;  char* X2; char* Gf; char* sT; unsigned* fl;
};
constexpr int FL_H = 64, FL_G = FL_H + 256 * 32, FL_S = FL_G + 256 * 32;

constexpr int KR = 2080;
constexpr int UPAD = 256;
constexpr int R1OFF = KR * 2 + 128;
constexpr int KT_B = R1OFF + KR * 2 + 32;
constexpr int U_B = (UPAD + L_) * 2;
constexpr int XR = 4 * WL * 4096;
constexpr int X_KT = XR, X_U = X_KT + 2 * KT_B, X_SC = X_U + 2 * U_B;
constexpr int SC_B = 8192 + 2 * TABN * 8;
constexpr int X_SY = X_SC + SC_B;
constexpr int X_B1 = X_SY + 128;
constexpr int X_DUM = X_B1 + 512;
constexpr int LDS_B = X_DUM + 256;
static_assert(LDS_B <= 160 * 1024 && SC_B >= 16384 && 2 * KT_B >= 16 * (1024 + 32), "LDS");

__device__ __attribute__((noinline)) void code_end_marker();

__global__ __launch_bounds__(512, 2) void k_mega(Args a STARG) {
  extern __shared__ __attribute__((aligned(16))) char lds[];
  const int tid = threadIdx.x, lane = tid & 63, w = __builtin_amdgcn_readfirstlane(tid >> 6);
  const int b = blockIdx.x, xg = b & 7, r_ = b >> 3;
  const int tb = xg * 32 + r_;
  const unsigned lbase = (unsigned)(size_t)(LDSP char*)lds;
  volatile LDSP unsigned* sy = (volatile LDSP unsigned*)(lds + X_SY);
  STDECL
  STW(0, 0, 0);
  const bool has1 = r_ < 28;
  const int idx1 = xg * 28 + (has1 ? r_ : 0);
  const int fT = idx1 >> 3, dT = idx1 & 7;
  const int sTl = tb >> 3, dT2 = tb & 7;
  u32x4 xr; unsigned ep;
  asm volatile("global_load_dwordx4 %0, %2, %3\n\tglobal_load_dword %1, %4, %5 sc1" : "=&v"(xr), "=&v"(ep)
               : "v"(lane * 16), "s"(a.x + (size_t)(tb * 8 + w) * 64), "v"(0), "s"(a.fl) : "memory");
  if (tid < 32) sy[tid] = 0;
  unsigned praw[8];
  if (w >= 4) {
    const int i = (tb * 2) * N_ + (w - 4) * 64;
    const int dch = tb * 2 + ((w - 4) >> 1);
    asm volatile("global_load_ushort %0, %8, %10\n\tglobal_load_ushort %1, %8, %11\n\tglobal_load_ushort %2, %8, %12\n\tglobal_load_ushort %3, %8, %13\n\t"
                 "global_load_ushort %4, %8, %14\n\tglobal_load_ushort %5, %8, %15\n\tglobal_load_ushort %6, %9, %16\n\tglobal_load_ushort %7, %9, %17\n\ts_barrier"
                 : "=&v"(praw[0]), "=&v"(praw[1]), "=&v"(praw[2]), "=&v"(praw[3]), "=&v"(praw[4]), "=&v"(praw[5]), "=&v"(praw[6]), "=&v"(praw[7])
                 : "v"(lane * 2), "v"(0), "s"(a.Are + i), "s"(a.Aim + i), "s"(a.Bre + i), "s"(a.Bim + i), "s"(a.Cre + i), "s"(a.Cim + i),
                   "s"(a.logdt + dch), "s"(a.Dskip + dch) : "memory");
    st16_dev(a.sT + (size_t)tb * 8192 + (w - 4) * 2048 + lane * 16, u32x4{~0u, ~0u, ~0u, ~0u});
    st16_dev(a.sT + (size_t)tb * 8192 + (w - 4) * 2048 + 1024 + lane * 16, u32x4{~0u, ~0u, ~0u, ~0u});
    const int li = r_ * 256 + tid - 256, ft0 = (xg * 28) >> 3;
    const char* t1 = (const char*)a.w1 + (size_t)(li >> 2) * (F_ * 2) + (ft0 + (li & 3)) * 128;
    const char* t2 = t1;
    {
      unsigned long long pc;
      asm volatile("s_getpc_b64 %0" : "=s"(pc));
      const unsigned long long pe = (unsigned long long)(void*)&code_end_marker;
      const unsigned long long pa = (pc & ~127ull) + (unsigned long long)(((w - 4) * 4 + (r_ & 3)) * 64 + lane) * 128;
      if (pe > pc && pe - pc < 65536 && pa + 4 <= pe && r_ >= 28 && w < 6) t2 = (const char*)pa;
    }
    asm volatile("s_mov_b32 m0, %2\n\ts_nop 0\n\tglobal_load_lds_dword %1, off\n\tglobal_load_lds_dword %0, off\n\ts_waitcnt vmcnt(12)"
                 :: "v"(t1), "v"(t2), "s"(lbase + X_DUM) : "memory");
  } else {
    u32x4 bv;
    asm volatile("global_load_dwordx4 %0, %1, %2\n\ts_barrier\n\ts_waitcnt vmcnt(0)"
                 : "=&v"(bv) : "v"((lane & 7) * 16), "s"(a.b1 + fT * 64) : "memory");
    if (lane < 8) *(u32x4*)(lds + X_B1 + w * 128 + lane * 16) = bv;
  }
  asm volatile("" : "+v"(xr), "+v"(ep));
  const unsigned tag = __builtin_amdgcn_readfirstlane(ep) + 1u;
  STW(0, 1, 0);
  constexpr int RS = 1024 + 32;
  {
    float v[8];
#pragma unroll
    for (int j = 0; j < 4; ++j) { v[2 * j] = bf_lo(xr[j]); v[2 * j + 1] = bf_hi(xr[j]); }
    u32x4 o;
    ln8<true>(v, o);
    *(u32x4*)(lds + X_KT + w * RS + lane * 16) = o;
    ln8<false>(v, o);
    *(u32x4*)(lds + X_KT + (8 + w) * RS + lane * 16) = o;
  }
  asm volatile("s_waitcnt vmcnt(2)" ::: "memory");
  lds_barrier();
  STW(0, 2, 0);

  if (w < 4) {
    __builtin_amdgcn_s_setprio(3);
    const int g = lane >> 4, c = lane & 15, q = (lane & 15) >> 2, p = lane & 3;
    {
      u32x4 ov[2][2];
#pragma unroll
      for (int arr = 1; arr >= 0; --arr)
#pragma unroll
        for (int it = 0; it < 2; ++it) {
          const char* base = lds + X_KT + (arr * 8 + q) * RS + (w * 8 + it * 4 + g) * 32 + p * 8;
          s16x4 lo = __builtin_amdgcn_ds_read_tr16_b64_v4i16((LDSP s16x4*)(base));
          s16x4 hi = __builtin_amdgcn_ds_read_tr16_b64_v4i16((LDSP s16x4*)(base + 4 * RS));
          ov[arr][it] = __builtin_bit_cast(u32x4, __builtin_shufflevector(lo, hi, 0, 1, 2, 3, 4, 5, 6, 7));
        }
#pragma unroll
      for (int arr = 1; arr >= 0; --arr)
#pragma unroll
        for (int it = 0; it < 2; ++it) {
          const int dt = w * 8 + it * 4 + g;
          if (arr) st16_dev(a.Hf + ((size_t)((dt >> 2) * 64 + kpos(tb >> 2)) * 4 + (dt & 3)) * 1024 + ((tb & 3) * 16 + c) * 16, ov[arr][it]);
          else st16_dev(a.X2 + ((size_t)tb * 512 + dt * 16 + c) * 16, ov[arr][it]);
        }
    }
    const unsigned lring = lbase + w * (WL * 4096);
    TrAddr tra;
    tr_addr(tra, lds + w * (WL * 4096), lane);
    f32x4 acc[4][4], res[4];
    unsigned vo[4];
    w_offsets<F_ * 2>(lane, vo);
    const char* wb1 = (const char*)a.w1 + (size_t)(w * 32) * (F_ * 2) + fT * 128;
    const char* wb2 = (const char*)a.w2 + (size_t)(w * 32) * (L_ * 2) + sTl * 128;
    unsigned long long tH = 0;
    bf16x8 wr2[RW2][4];
    if (has1) { w_issue<F_ * 2, 0>(wb1, vo, lring, 0, E1); asm volatile("s_waitcnt vmcnt(%0)" :: "i"(4 * E1) : "memory"); }
    else asm volatile("s_waitcnt vmcnt(0)" ::: "memory");
    grp_arrive(sy + 12, w, 1, lane);
    if (w == 0) {
      grp_wait<SPF>(sy + 12, 1);
      if (lane == 0) { st4_dev(a.fl + FL_H + tb * HSP, tag); sy[8] = 1; }
    } else word_wait<SPF>(sy + 8, 1);
    __builtin_amdgcn_s_setprio(0);
    STW(1, 0, 0);
    if (has1) {
      bf16x8 wr1[RW1][4];
      w_issue<F_ * 2, 0>(wb1, vo, lring, E1, WL);
      asm volatile("s_waitcnt vmcnt(%0)" :: "i"(4 * (WL - RW1)) : "memory");
      w_to_regs<RW1, 0>(tra, wr1);
      w_issue<F_ * 2, 0>(wb1, vo, lring, WL, WL + RW1);
      STW(1, 1, 0);
      if (w == 0) {
        poll_flags4(a.fl + FL_H + lane * 4 * HSP, tag);
        if (lane == 0) sy[11] = 1;
      } else word_wait<1>(sy + 11, 1);
      __builtin_amdgcn_s_setprio(3);
      STW(1, 2, 0);
      gemm_main<16, F_ * 2, false, RW1, 0>(a.Hf + (size_t)(dT * 64 + w) * 4096, a.Hf + (size_t)(dT * 64 + (w ^ 2)) * 4096, wb1, vo, lring, tra, wr1, w, lane, acc, lds + w * (WL * 4096) + lane * 16, sy + 19
      );
      STW(1, 3, 0);
      w_offsets<L_ * 2>(lane, vo);
      reduce4(acc, res, lds, w, lane, sy, 1);
      STW(1, 4, 0);
      u32x2 o[4];
#pragma unroll
      for (int j = 0; j < 4; ++j) {
        const u32x2 bb = *(const u32x2*)(lds + X_B1 + w * 128 + (16 * j + 4 * g) * 2);
        const float v0 = gelu_tanh(res[j][0] + bf_lo(bb[0])), v1 = gelu_tanh(res[j][1] + bf_hi(bb[0]));
        const float v2 = gelu_tanh(res[j][2] + bf_lo(bb[1])), v3 = gelu_tanh(res[j][3] + bf_hi(bb[1]));
        o[j] = u32x2{pk_bf16(v0, v1), pk_bf16(v2, v3)};
      }
#pragma unroll
      for (int jp = 0; jp < 2; ++jp) {
        u32x4 q4;
#pragma unroll
        for (int e = 0; e < 2; ++e) {
          auto sw = __builtin_amdgcn_permlane16_swap(o[2 * jp][e], o[2 * jp + 1][e], false, false);
          q4[e] = sw[0]; q4[2 + e] = sw[1];
        }
        const int f = fT * 64 + 16 * (2 * jp + (g & 1)) + 8 * (g >> 1);
        st16_dev(a.Gf + ((size_t)(dT * 56 + kpos(f >> 5)) * 4 + w) * 1024 + (((f & 31) >> 3) * 16 + c) * 16, q4);
      }
      grp_arrive(sy + 24, w, 1, lane);
      word_wait<SPF>(sy + 28 + w, 1);
      w_to_regs<RW2, 3>(tra, wr2);
      asm volatile("s_waitcnt vmcnt(0)" ::: "memory");
      grp_arrive(sy + 12, w, 2, lane);
      if (w == 0) {
        grp_wait<SPF>(sy + 12, 2);
        if (lane == 0) { st4_dev(a.fl + FL_G + (dT * 32 + fT) * GSP, tag); sy[9] = 1; }
      } else word_wait<SPF>(sy + 9, 1);
      __builtin_amdgcn_s_setprio(0);
      STW(1, 5, 0);
    } else {
      const int bq = r_ - 28;
#pragma unroll 1
      for (int li = tid; li < 1792; li += 256) {
        if (li - tid == PFA * 256) {
          asm volatile("s_waitcnt vmcnt(0)" ::: "memory");
          if (w == 0) {
            __builtin_amdgcn_s_sleep(HDEL);
            poll_flags4(a.fl + FL_H + lane * 4 * HSP, tag);
            if (lane == 0) sy[11] = 1;
            tH = __builtin_amdgcn_s_memrealtime();
          }
          grp_wait<2>(sy + 4, 2);
        }
        touch(lbase + X_DUM, (const char*)a.w2 + (size_t)((li >> 2) * 4 + bq) * (L_ * 2) + (xg * 4 + (li & 3)) * 128);
        asm volatile("s_waitcnt vmcnt(3)" ::: "memory");
      }
      w_offsets<L_ * 2>(lane, vo);
      w_issue<L_ * 2, 3>(wb2, vo, lring, 0, RW2);
      asm volatile("s_waitcnt vmcnt(0)" ::: "memory");
      w_to_regs<RW2, 3>(tra, wr2);
      STW(1, 6, 0);
    }
    static_assert(RW2 <= WL - 3, "");
    w_issue<L_ * 2, 3>(wb2, vo, lring, RW2, RW2 + WL);
    STW(2, 0, 0);
    if (w == 0) {
      if (!has1) {
#pragma unroll 1
        for (int sp = 0; sp < 4096 && __builtin_amdgcn_s_memrealtime() - tH < IDEL; ++sp) __builtin_amdgcn_s_sleep(4);
      }
      poll_flags<PBK>(a.fl + FL_G + (dT2 * 32 + (lane & 31)) * GSP, tag, lane >= 28);
      if (lane == 0) sy[11] = 2;
    } else word_wait<1>(sy + 11, 2);
    STW(2, 1, 0);
    __builtin_amdgcn_s_setprio(3);
    gemm_main<14, L_ * 2, true, RW2, 3>(a.Gf + (size_t)(dT2 * 56 + w) * 4096, a.Gf + (size_t)(dT2 * 56 + (w ^ 2)) * 4096, wb2, vo, lring, tra, wr2, w, lane, acc, lds + w * (WL * 4096) + lane * 16, sy + 19
      );
    STW(2, 2, 0);
    u32x2 xv[4];
    unsigned braw[4];
    const size_t oo = (size_t)(sTl * 64 + c) * D_ + dT2 * 64 + 16 * w + 4 * g;
#pragma unroll
    for (int j = 0; j < 4; ++j) {
      braw[j] = a.b2[sTl * 64 + 16 * j + c];
      xv[j] = *(const u32x2*)((cus)a.x + oo + (size_t)(16 * j) * D_);
    }
    reduce4(acc, res, lds, w, lane, sy, 2);
    STW(2, 3, 0);
    word_wait<SPF>(sy + 10, 1);
    STW(2, 4, 0);
#pragma unroll
    for (int j = 0; j < 4; ++j) {
      const char* ap = lds + X_SC + (16 * w + 4 * g + q) * 144 + j * 32 + p * 8;
      const u32x2 sv = __builtin_bit_cast(u32x2, __builtin_amdgcn_ds_read_tr16_b64_v4i16((LDSP s16x4*)ap));
      const float bias = bf_lo(braw[j]);
      const float o0 = bf_lo(xv[j][0]) + bf_lo(sv[0]) * silu(res[j][0] + bias);
      const float o1 = bf_hi(xv[j][0]) + bf_hi(sv[0]) * silu(res[j][1] + bias);
      const float o2 = bf_lo(xv[j][1]) + bf_lo(sv[1]) * silu(res[j][2] + bias);
      const float o3 = bf_hi(xv[j][1]) + bf_hi(sv[1]) * silu(res[j][3] + bias);
      u32x2 o = {pk_bf16(o0, o1), pk_bf16(o2, o3)};
      *(u32x2*)(a.out + oo + (size_t)(16 * j) * D_) = o;
    }
    STW(2, 5, 0);
    STDUMP(0);
  } else {
    const int j4 = w - 4;
    volatile LDSP unsigned* xb = sy + 4;
    asm volatile("s_waitcnt vmcnt(2)" : "+v"(praw[0]), "+v"(praw[1]), "+v"(praw[2]), "+v"(praw[3]), "+v"(praw[4]), "+v"(praw[5]), "+v"(praw[6]), "+v"(praw[7]) :: "memory");
    const int ch = j4 >> 1, hw = j4 & 1;
    const int d = tb * 2 + ch;
    {
      float* par = (float*)(lds + X_SC + j4 * 2048);
      float* tabf = (float*)(lds + X_SC + 8192 + ch * (TABN * 8));
      const float dt = rbf(__expf(bf_lo(praw[6])));
      const float zr = dt * bf_lo(praw[0]) * LOG2E, zi = dt * bf_lo(praw[1]) * 0.15915494309189535f;
      const float br = dt * bf_lo(praw[2]), bi = dt * bf_lo(praw[3]), cr = bf_lo(praw[4]), ci = bf_lo(praw[5]);
      float wr = cr * br - ci * bi, wi = cr * bi + ci * br;
      const float zmax = wave_max_nn(fmaxf(zr, 0.f));
      const float wmax = wave_max_nn(fmaxf(fabsf(wr), fabsf(wi)));
      const unsigned we = (__float_as_uint(wmax) >> 23) & 0xff;
      const unsigned wec = we < 16 ? 16 : (we > 250 ? 250 : we);
      const float wsc = __uint_as_float((266u - wec) << 23);
      const float osc = __uint_as_float((wec - 12u) << 23) * 6.103515625e-05f;
      const float zs = zr - zmax;
      wr *= wsc; wi *= wsc;
      float qr, qi, er, ei;
      cexp2(zs * 128.f, zi * 128.f, qr, qi);
      cexp2(zs * 1024.f, zi * 1024.f, er, ei);
      *(f32x4*)(par + lane * 8) = f32x4{zs, zi, wr, wi};
      *(f32x4*)(par + lane * 8 + 4) = f32x4{qr, qi, er, ei};
      const float p2r = er * er - ei * ei, p2i = 2.f * er * ei;
      const float k2048 = wave_sum(wr * p2r - wi * p2i) * 16384.f;
      STW(3, 0, 4);
      const int r = lane & 15, g = lane >> 4;
      const float va = 8.f * r;
      const int off = r < 8 ? r : r < 12 ? 256 + 2 * (r - 8) : 512 + 256 * ((r - 12) >> 1) + 4 * (r & 1);
      const float vb = (float)off;
      f32x4 acc[2][2];
#pragma unroll
      for (int t = 0; t < 4; ++t) acc[t >> 1][t & 1] = f32x4{0.f, 0.f, 0.f, 0.f};
#pragma unroll 1
      for (int ks = 0; ks < 4; ++ks) {
        u32x4 a0, a1, b0, b1;
#pragma unroll
        for (int j = 0; j < 4; ++j) {
          const int n = ks * 16 + g * 4 + j;
          const f32x4 pp = *(const f32x4*)(par + n * 8), pq = *(const f32x4*)(par + n * 8 + 4);
          float tr, ti, xr_, xi_;
          cexp2(__builtin_fmaf(pp[0], va, 14.f), pp[1] * va, tr, ti);
          cexp2(pp[0] * vb, pp[1] * vb, xr_, xi_);
          const float ur = pp[2] * xr_ - pp[3] * xi_, ui = pp[2] * xi_ + pp[3] * xr_;
          a0[j] = pk_f16(tr, ti);
          a1[j] = pk_f16(tr * pq[0] - ti * pq[1], tr * pq[1] + ti * pq[0]);
          b0[j] = pk_f16(ur, -ui);
          b1[j] = pk_f16(ur * pq[2] - ui * pq[3], -(ur * pq[3] + ui * pq[2]));
        }
        acc[0][0] = __builtin_amdgcn_mfma_f32_16x16x32_f16(__builtin_bit_cast(f16x8, a0), __builtin_bit_cast(f16x8, b0), acc[0][0], 0, 0, 0);
        acc[1][0] = __builtin_amdgcn_mfma_f32_16x16x32_f16(__builtin_bit_cast(f16x8, a1), __builtin_bit_cast(f16x8, b0), acc[1][0], 0, 0, 0);
        acc[0][1] = __builtin_amdgcn_mfma_f32_16x16x32_f16(__builtin_bit_cast(f16x8, a0), __builtin_bit_cast(f16x8, b1), acc[0][1], 0, 0, 0);
        acc[1][1] = __builtin_amdgcn_mfma_f32_16x16x32_f16(__builtin_bit_cast(f16x8, a1), __builtin_bit_cast(f16x8, b1), acc[1][1], 0, 0, 0);
      }
      STW(3, 1, 4);
      float kl = k2048 * osc;
#pragma unroll
      for (int t = 0; t < 4; ++t) acc[t >> 1][t & 1] *= osc;
      if (zmax > 0.f) {
#pragma unroll
        for (int t = 0; t < 4; ++t)
#pragma unroll
          for (int e = 0; e < 4; ++e)
            acc[t >> 1][t & 1][e] *= __builtin_amdgcn_exp2f(zmax * (vb + (float)(8 * (16 * (t >> 1) + e) + 1024 * (t & 1)) + 32.f * g));
        kl *= __builtin_amdgcn_exp2f(zmax * 2048.f);
      }
      {
        const int stride = r < 8 ? 8 : r < 12 ? 4 : 2;
        const int base = r < 8 ? r : r < 12 ? 256 + (r - 8) : 384 + 64 * ((r - 12) >> 1) + (r & 1);
        float* p0 = tabf + (base + stride * 4 * g) * 2 + hw;
#pragma unroll
        for (int it = 0; it < 2; ++it)
#pragma unroll
          for (int e = 0; e < 4; ++e) p0[stride * (16 * it + e) * 2] = acc[it][0][e];
        if ((off & 255) == 0) {
          float* p1 = tabf + (512 + (off >> 3) + 4 * g) * 2 + hw;
#pragma unroll
          for (int it = 0; it < 2; ++it)
#pragma unroll
            for (int e = 0; e < 4; ++e) p1[(16 * it + e) * 2] = acc[it][1][e];
        }
        if (lane < 2) tabf[(640 + lane) * 2 + hw] = lane ? 0.f : kl;
      }
      grp_arrive(xb, j4, 1, lane);
      const int l2 = hw * 64 + lane;
      char* lk = lds + X_KT + ch * KT_B;
      int ix[17];
#pragma unroll
      for (int e = 0; e < 17; e += 2) {
        const int j0 = 2047 - 16 * l2 - e, j1 = j0 - 1;
        const unsigned bb = pk_bf16((float)j0, (float)j1);
        const int i0 = (int)(bb & 0xffff) - (0x4300 - 128), i1 = (int)(bb >> 16) - (0x4300 - 128);
        ix[e] = j0 < 128 ? (j0 < 0 ? 641 : j0) : i0;
        if (e + 1 < 17) ix[e + 1] = j1 < 128 ? j1 : i1;
      }
      word_wait<1>(sy + 8, 1);
      if (l2 < 8) {
        const int pc = (l2 & 2) ? 258 + (l2 & 1) : (l2 & 1);
        *(u32x4*)(lk + (l2 >> 2) * R1OFF + pc * 16) = u32x4{0, 0, 0, 0};
      }
      if (j4 == 0) *(u32x4*)(lds + X_U + (lane & 1) * U_B + (lane >> 1) * 16) = u32x4{0, 0, 0, 0};
#pragma unroll
      for (int e = 0; e < 17; ++e) asm volatile("" : "+v"(ix[e]));
      grp_wait<0>(xb, 1);
      f32x2 tv[17];
#pragma unroll
      for (int e = 0; e < 17; ++e) tv[e] = *(const f32x2*)(tabf + ix[e] * 2);
      __builtin_amdgcn_sched_barrier(0);
      float val[17];
#pragma unroll
      for (int e = 0; e < 17; ++e) val[e] = tv[e][0] + tv[e][1];
#pragma unroll
      for (int hf = 0; hf < 2; ++hf) {
        u32x4 o0, o1;
#pragma unroll
        for (int e = 0; e < 4; ++e) { o0[e] = pk_bf16(val[8 * hf + 2 * e], val[8 * hf + 2 * e + 1]); o1[e] = pk_bf16(val[8 * hf + 2 * e + 1], val[8 * hf + 2 * e + 2]); }
        *(u32x4*)(lk + 32 + l2 * 32 + hf * 16) = o0;
        *(u32x4*)(lk + R1OFF + 32 + l2 * 32 + hf * 16) = o1;
      }
      if (l2 == 0) *(unsigned*)(lk + R1OFF + 28) = pk_bf16(0.f, val[0]);
      STW(3, 2, 4);
    }
    word_wait(sy + 11, 1);
    STW(3, 3, 4);
    {
      u32x4 u0, u1;
      const int t0 = j4 * 64 + (lane >> 1);
      asm volatile("global_load_dwordx4 %0, %2, off sc1\n\tglobal_load_dwordx4 %1, %3, off sc1\n\ts_waitcnt vmcnt(0)" : "=&v"(u0), "=&v"(u1)
                   : "v"(a.X2 + ((size_t)t0 * 512 + tb * 2 + (lane & 1)) * 16), "v"(a.X2 + ((size_t)(t0 + 32) * 512 + tb * 2 + (lane & 1)) * 16) : "memory");
      char* lu = lds + X_U + (lane & 1) * U_B;
      *(u32x4*)(lu + UPAD * 2 + t0 * 16) = u0;
      *(u32x4*)(lu + UPAD * 2 + (t0 + 32) * 16) = u1;
    }
    grp_bar(xb, j4, 2, lane);
    if (b == 0 && tid == 256) st4_dev(a.fl, tag);
    STW(3, 8, 4);
    {
      const float dsk = bf_lo(praw[7]);
      const char* lk = lds + X_KT + ch * KT_B;
      const char* lu = lds + X_U + ch * U_B;
      const int col = lane & 15, g = lane >> 4;
      const int p00 = 2047 - col + 8 * g;
      const char* gbase = lk + ((p00 & 1) ? R1OFF : 0) + (p00 >> 1) * 4 - 256 * hw;
      const char* bbase = lu + (UPAD - 16 + 16 * col + 8 * g) * 2 - 256 * hw;
      f32x4 acc[8];
#pragma unroll
      for (int i = 0; i < 8; ++i) acc[i] = f32x4{0.f, 0.f, 0.f, 0.f};
      bf16x8 bf[2][8], gf[2][8];
#define CLD(set, rr) do { _Pragma("unroll") for (int i = 0; i < 8; ++i) { \
          bf[set][i] = *(const bf16x8*)(bbase + 64 * (8 * i - (rr))); \
          const unsigned* gp = (const unsigned*)(gbase - 64 * (8 * i + (rr))); \
          u32x4 gv = {gp[0], gp[1], gp[2], gp[3]}; gf[set][i] = __builtin_bit_cast(bf16x8, gv); } } while (0)
      CLD(0, 0);
      CLD(1, 1);
      if (has1) {
        unsigned vo2[4];
        w_offsets<L_ * 2>(lane, vo2);
        word_wait<1>(sy + j4, 1);
        w_issue<L_ * 2, 3>((const char*)a.w2 + (size_t)(j4 * 32) * (L_ * 2) + sTl * 128, vo2, lbase + j4 * (WL * 4096), 0, RW2);
        asm volatile("s_waitcnt vmcnt(0)" ::: "memory");
        if (lane == 0) sy[28 + j4] = 1;
      }
      if (CGATE == 1 && has1) word_wait<2>(sy + 9, 1);
      if (CGATE == 2 && has1) grp_wait<1>(sy + 24, 1);
      STW(3, 4, 4);
#pragma unroll
      for (int rr = 0; rr < 4; ++rr) {
#pragma unroll
        for (int bq = 0; bq < 8; ++bq)
#pragma unroll
          for (int i = 0; i + bq < 8; ++i) acc[i + bq] = __builtin_amdgcn_mfma_f32_16x16x32_bf16(gf[rr & 1][bq], bf[rr & 1][i], acc[i + bq], 0, 0, 0);
        asm volatile("" : "+v"(acc[0]), "+v"(acc[1]), "+v"(acc[2]), "+v"(acc[3]), "+v"(acc[4]), "+v"(acc[5]), "+v"(acc[6]), "+v"(acc[7]) :: "memory");
        if (rr + 2 < 4) CLD(rr & 1, rr + 2);
        __builtin_amdgcn_sched_barrier(0);
      }
#undef CLD
      __builtin_amdgcn_s_setprio(XPR);
      STW(3, 5, 4);
      float* xc = (float*)(lds + X_SC + ch * 8192);
      f32x4 keep[4];
#pragma unroll
      for (int i = 0; i < 4; ++i) {
        f32x4 give;
#pragma unroll
        for (int e = 0; e < 4; ++e) { give[e] = hw ? acc[2 * i][e] : acc[2 * i + 1][e]; keep[i][e] = hw ? acc[2 * i + 1][e] : acc[2 * i][e]; }
        *(f32x4*)(xc + ((hw * 4 + i) * 64 + lane) * 4) = give;
      }
      grp_bar<0>(xb, j4, 3, lane);
      u32x2 o[4];
#pragma unroll
      for (int i = 0; i < 4; ++i) {
        const f32x4 y = keep[i] + *(const f32x4*)(xc + (((hw ^ 1) * 4 + i) * 64 + lane) * 4);
        const int t = 256 * (2 * i + hw) + 16 * col + 4 * g;
        const u32x2 uu = *(const u32x2*)(lu + (UPAD + t) * 2);
        o[i] = u32x2{pk_bf16(y[0] + bf_lo(uu[0]) * dsk, y[1] + bf_hi(uu[0]) * dsk), pk_bf16(y[2] + bf_lo(uu[1]) * dsk, y[3] + bf_hi(uu[1]) * dsk)};
      }
#pragma unroll
      for (int ip = 0; ip < 2; ++ip) {
        u32x4 q4;
#pragma unroll
        for (int e = 0; e < 2; ++e) {
          auto sw = __builtin_amdgcn_permlane16_swap(o[2 * ip][e], o[2 * ip + 1][e], false, false);
          q4[e] = sw[0]; q4[2 + e] = sw[1];
        }
        const int t = 256 * (2 * (2 * ip + (g & 1)) + hw) + 16 * col + 8 * (g >> 1);
        st16_dev(a.sT + ((size_t)d * L_ + t) * 2, q4);
      }
      grp_arrive(xb, j4, 4, lane);
      STW(3, 6, 4);
    }
    {
      __builtin_amdgcn_s_setprio(0);
      word_wait<2>(sy + 19, 1);
      grp_wait<1>(xb, 4);
      __builtin_amdgcn_s_setprio(XPR);
      u32x4 s0, s1;
      const int rw_ = j4 * 16 + (lane >> 3);
      const char* sp = a.sT + ((size_t)(dT2 * 64 + rw_) * L_ + sTl * 64) * 2 + (lane & 7) * 16;
#pragma unroll 1
      for (int att = 0; att < 4096; ++att) {
        asm volatile("global_load_dwordx4 %0, %2, off sc1\n\tglobal_load_dwordx4 %1, %3, off sc1\n\ts_waitcnt vmcnt(0)" : "=&v"(s0), "=&v"(s1)
                     : "v"(sp), "v"(sp + (size_t)8 * L_ * 2) : "memory");
        const unsigned mx = max(max(max(s0[0], s0[1]), max(s0[2], s0[3])), max(max(s1[0], s1[1]), max(s1[2], s1[3])));
        if (!__any(mx == ~0u)) break;
        __builtin_amdgcn_s_sleep(SBK);
      }
      *(u32x4*)(lds + X_SC + rw_ * 144 + (lane & 7) * 16) = s0;
      *(u32x4*)(lds + X_SC + (rw_ + 8) * 144 + (lane & 7) * 16) = s1;
      grp_bar(xb, j4, 5, lane);
      if (tid == 256) sy[10] = 1;
      STW(3, 7, 4);
      STDUMP(4);
    }
  }
}

__device__ __attribute__((noinline)) void code_end_marker() { asm volatile("s_nop 0"); }

extern "C" void kernel_launch(void* const* d_in, const int* in_sizes, int n_in, void* d_out, int out_size, void* d_ws,
                              size_t ws_size, hipStream_t stream) {
  char* ws = (char*)d_ws;
  Args a;
  a.x = (const u32x4*)d_in[0];
  a.Are = (cus)d_in[1]; a.Aim = (cus)d_in[2]; a.Bre = (cus)d_in[3]; a.Bim = (cus)d_in[4]; a.Cre = (cus)d_in[5]; a.Cim = (cus)d_in[6];
  a.logdt = (cus)d_in[7]; a.Dskip = (cus)d_in[8];
  a.w1 = (cus)d_in[10]; a.b1 = (cus)d_in[11]; a.w2 = (cus)d_in[12]; a.b2 = (cus)d_in[13];
  a.out = (unsigned short*)d_out;
  a.X2 = ws; a.Hf = ws + (2u << 20); a.Gf = ws + (4u << 20); a.sT = ws + (6u << 20); a.fl = (unsigned*)(ws + (8u << 20));
  (void)hipFuncSetAttribute((const void*)k_mega, hipFuncAttributeMaxDynamicSharedMemorySize, LDS_B);
  k_mega<<<256, 512, LDS_B, stream>>>(a STPASS);
}
```

```cpp
#include <hip/hip_runtime.h>
#include <hip/hip_bf16.h>
#include <stdint.h>

typedef short s16x4 __attribute__((ext_vector_type(4)));
typedef short bf16x8 __attribute__((ext_vector_type(8)));
typedef _Float16 f16x8 __attribute__((ext_vector_type(8)));
typedef _Float16 h2_t __attribute__((ext_vector_type(2)));
typedef __bf16 bf2_t __attribute__((ext_vector_type(2)));
typedef float f32x2 __attribute__((ext_vector_type(2)));
typedef float f32x4 __attribute__((ext_vector_type(4)));
typedef unsigned u32x2 __attribute__((ext_vector_type(2)));
typedef unsigned u32x4 __attribute__((ext_vector_type(4)));
typedef const unsigned short* cus;
#define DI __device__ __forceinline__
#define LDSP __attribute__((address_space(3)))


#define RW1 5
#define RW2 5
#define WL 7
#define AD 3
#define XS2 4
#define PBK 6
#define GSP 32
#define GSTEP (16 - AD)
#define PSTEP 6
#define IDEL 800

constexpr int L_ = 2048, D_ = 512, N_ = 128, F_ = 1792;
constexpr int TABN = 648;
constexpr float LOG2E = 1.44269504088896341f;
constexpr int SPIN_CAP = 1 << 18;

DI unsigned pk_bf16(float lo, float hi) {
  f32x2 f = {lo, hi};
  return __builtin_bit_cast(unsigned, __builtin_convertvector(f, bf2_t));
}
DI unsigned pk_f16(float lo, float hi) {
  f32x2 f = {lo, hi};
  return __builtin_bit_cast(unsigned, __builtin_convertvector(f, h2_t));
}
DI float bf_lo(unsigned u) { return __uint_as_float(u << 16); }
DI float bf_hi(unsigned u) { return __uint_as_float(u & 0xffff0000u); }
DI float rbf(float x) { return bf_lo(pk_bf16(x, 0.f)); }

template <int CTRL, int RM, bool BC> DI float dppf(float v) {
  return __builtin_bit_cast(float, __builtin_amdgcn_update_dpp(0, __builtin_bit_cast(int, v), CTRL, RM, 0xf, BC));
}
DI float wave_sum(float v) {
  v += dppf<0x111, 0xf, true>(v);
  v += dppf<0x112, 0xf, true>(v);
  v += dppf<0x114, 0xf, true>(v);
  v += dppf<0x118, 0xf, true>(v);
  v += dppf<0x142, 0xa, false>(v);
  v += dppf<0x143, 0xc, false>(v);
  return __builtin_bit_cast(float, __builtin_amdgcn_readlane(__builtin_bit_cast(int, v), 63));
}
DI float wave_max_nn(float v) {
  v = fmaxf(v, dppf<0x111, 0xf, true>(v));
  v = fmaxf(v, dppf<0x112, 0xf, true>(v));
  v = fmaxf(v, dppf<0x114, 0xf, true>(v));
  v = fmaxf(v, dppf<0x118, 0xf, true>(v));
  v = fmaxf(v, dppf<0x142, 0xa, false>(v));
  v = fmaxf(v, dppf<0x143, 0xc, false>(v));
  return __builtin_bit_cast(float, __builtin_amdgcn_readlane(__builtin_bit_cast(int, v), 63));
}
DI void touch(unsigned ldum, const void* p) { asm volatile("s_mov_b32 m0, %0\n\ts_nop 0\n\tglobal_load_lds_dword %1, off" :: "s"(ldum), "v"(p) : "memory"); }
DI void lds_barrier() { asm volatile("s_waitcnt lgkmcnt(0)" ::: "memory"); __builtin_amdgcn_s_barrier(); asm volatile("" ::: "memory"); }
DI void st16_dev(void* p, u32x4 v) { asm volatile("global_store_dwordx4 %0, %1, off sc1\n\ts_nop 1" :: "v"(p), "v"(v) : "memory"); }
DI void st4_dev(void* p, unsigned v) { asm volatile("global_store_dword %0, %1, off sc1" :: "v"(p), "v"(v) : "memory"); }
DI unsigned ld4_dev_wait(const void* p) { unsigned r; asm volatile("global_load_dword %0, %1, off sc1\n\ts_waitcnt vmcnt(0)" : "=&v"(r) : "v"(p) : "memory"); return r; }

DI void grp_arrive(volatile LDSP unsigned* words, int idx, unsigned ph, int lane) {
  asm volatile("s_waitcnt lgkmcnt(0)" ::: "memory");
  if (lane == 0) words[idx] = ph;
}
template <int SL = 1>
DI void grp_wait(volatile LDSP unsigned* words, unsigned ph) {
#pragma unroll 1
  for (int sp = 0; sp < SPIN_CAP * 8; ++sp) {
    const u32x4 v = *(volatile LDSP u32x4*)words;
    const unsigned m = min(min(v[0], v[1]), min(v[2], v[3]));
    if (__builtin_amdgcn_readfirstlane(m) >= ph) break;
    if (SL) __builtin_amdgcn_s_sleep(SL);
  }
  asm volatile("" ::: "memory");
}
template <int SL = 1>
DI void grp_bar(volatile LDSP unsigned* words, int idx, unsigned ph, int lane) { grp_arrive(words, idx, ph, lane); grp_wait<SL>(words, ph); }
template <int SL = 1>
DI void word_wait(volatile LDSP unsigned* word, unsigned v) {
#pragma unroll 1
  for (int sp = 0; sp < SPIN_CAP * 8; ++sp) {
    if (__builtin_amdgcn_readfirstlane(*word) >= v) break;
    if (SL) __builtin_amdgcn_s_sleep(SL);
  }
  asm volatile("" ::: "memory");
}
template <int BK = PBK>
DI void poll_flags(const unsigned* p, unsigned tag, bool done) {
#pragma unroll 1
  for (int sp = 0; sp < SPIN_CAP; ++sp) {
    if (!done) done = ld4_dev_wait(p) == tag;
    if (__all(done)) break;
    __builtin_amdgcn_s_sleep(BK);
  }
}
DI void poll_flags4(const unsigned* p, unsigned tag) {
  bool done = false;
#pragma unroll 1
  for (int sp = 0; sp < SPIN_CAP; ++sp) {
    if (!done) {
      u32x4 f;
      asm volatile("global_load_dwordx4 %0, %1, off sc1\n\ts_waitcnt vmcnt(0)" : "=&v"(f) : "v"(p) : "memory");
      done = f[0] == tag && f[1] == tag && f[2] == tag && f[3] == tag;
    }
    if (__all(done)) break;
    __builtin_amdgcn_s_sleep(PBK);
  }
}

DI int kpos(int kb) { return kb ^ (((kb >> 2) & 1) << 1); }

template <bool PIVOT>
DI void ln8(const u32x4& in, u32x4& o) {
  f32x2 v[4];
#pragma unroll
  for (int i = 0; i < 4; ++i) v[i] = f32x2{bf_lo(in[i]), bf_hi(in[i])};
  const float p = PIVOT ? __builtin_bit_cast(float, __builtin_amdgcn_readfirstlane(__builtin_bit_cast(int, v[0][0]))) : 0.f;
  f32x2 t[4];
#pragma unroll
  for (int i = 0; i < 4; ++i) t[i] = PIVOT ? v[i] - p : v[i];
  const f32x2 s2 = (t[0] + t[1]) + (t[2] + t[3]);
  const f32x2 q2 = __builtin_elementwise_fma(t[0], t[0], t[1] * t[1]) + __builtin_elementwise_fma(t[2], t[2], t[3] * t[3]);
  const float ds = wave_sum(s2[0] + s2[1]) * (1.f / D_), dq = wave_sum(q2[0] + q2[1]) * (1.f / D_);
  const float mean = p + ds, var = fmaxf(__builtin_fmaf(-ds, ds, dq), 0.f);
  const float mb = rbf(mean), vb = rbf(var);
  const float den = rbf(__builtin_amdgcn_sqrtf(rbf(vb + 1e-5f)));
  const float inv = __builtin_amdgcn_rcpf(den);
#pragma unroll
  for (int i = 0; i < 4; ++i) {
    const f32x2 c = v[i] - mb;
    const unsigned r = pk_bf16(c[0], c[1]);
    const f32x2 n = f32x2{bf_lo(r), bf_hi(r)} * inv;
    o[i] = pk_bf16(n[0], n[1]);
  }
}
DI void cexp2(float le, float ph, float& re, float& im) {
  const float e = __builtin_amdgcn_exp2f(le);
  const float p = __builtin_amdgcn_fractf(ph);
  re = e * __builtin_amdgcn_cosf(p);
  im = e * __builtin_amdgcn_sinf(p);
}
DI f32x2 xsig2(f32x2 x, f32x2 e) {
  const f32x2 d = f32x2{__builtin_amdgcn_exp2f(e[0]), __builtin_amdgcn_exp2f(e[1])} + 1.f;
  return x * f32x2{__builtin_amdgcn_rcpf(d[0]), __builtin_amdgcn_rcpf(d[1])};
}
DI f32x2 gelu_tanh2(f32x2 x) {
  constexpr float k1 = -2.f * 0.7978845608028654f * LOG2E, k2 = k1 * 0.044715f;
  return xsig2(x, x * __builtin_elementwise_fma(x * x, f32x2{k2, k2}, f32x2{k1, k1}));
}
DI f32x2 silu2(f32x2 x) { return xsig2(x, x * -LOG2E); }
DI f32x2 bf_pair(unsigned u) { return f32x2{bf_lo(u), bf_hi(u)}; }

#define DMA4(ldsaddr, vo, sb) do { unsigned keep_; \
    asm volatile("s_mov_b32 %0, m0\n\ts_mov_b32 m0, %1\n\ts_nop 0\n\t" \
                 "global_load_lds_dwordx4 %2, %6\n\tglobal_load_lds_dwordx4 %3, %6 offset:1024\n\t" \
                 "global_load_lds_dwordx4 %4, %6 offset:2048\n\tglobal_load_lds_dwordx4 %5, %6 offset:3072\n\ts_mov_b32 m0, %0" \
                 : "=&s"(keep_) : "s"(__builtin_amdgcn_readfirstlane(ldsaddr)), "v"(vo[0]), "v"(vo[1]), "v"(vo[2]), "v"(vo[3]), "s"(sb) : "memory"); } while (0)
template <int RB> DI void w_offsets(int lane, unsigned (&vo)[4]) {
  const int lr = lane >> 3, p8 = lane & 7;
#pragma unroll
  for (int j = 0; j < 4; ++j) vo[j] = (8 * j + lr) * RB + ((p8 ^ ((lr & 2) | ((j & 1) << 2))) << 4) - 1024 * j;
}
struct TrAddr { const char* p[4]; };
DI void tr_addr(TrAddr& t, const char* ring, int lane) {
  const int g = lane >> 4, q = (lane & 15) >> 2, p = lane & 3;
  const int frow = (q & 2) | ((g & 1) << 2);
  const char* trb = ring + (8 * g + q) * 128 + (p & 1) * 8;
#pragma unroll
  for (int j = 0; j < 4; ++j) t.p[j] = trb + (((2 * j + (p >> 1)) ^ frow) << 4);
}
DI void tr_read(bf16x8 (&fr)[4], const TrAddr& t, int off) {
#pragma unroll
  for (int j = 0; j < 4; ++j) {
    s16x4 lo = __builtin_amdgcn_ds_read_tr16_b64_v4i16((LDSP s16x4*)(t.p[j] + off));
    s16x4 hi = __builtin_amdgcn_ds_read_tr16_b64_v4i16((LDSP s16x4*)(t.p[j] + off + 512));
    fr[j] = __builtin_shufflevector(lo, hi, 0, 1, 2, 3, 4, 5, 6, 7);
  }
}
template <int RB, int OFF>
DI void w_issue(const char* wb, const unsigned (&vo)[4], unsigned lring, int k0, int k1) {
#pragma unroll 1
  for (int k = k0; k < k1; ++k) DMA4(lring + ((k + OFF) % WL) * 4096, vo, wb + (size_t)k * 128 * RB);
}
template <int RWN, int OFF, int K0 = 0, int K1 = RWN>
DI void w_to_regs(const TrAddr& tra, bf16x8 (&wr)[RWN][4]) {
#pragma unroll
  for (int k = K0; k < K1; ++k) tr_read(wr[k], tra, ((k + OFF) % WL) * 4096);
#pragma unroll
  for (int k = K0; k < K1; ++k) asm volatile("s_waitcnt lgkmcnt(0)" : "+v"(wr[k][0]), "+v"(wr[k][1]), "+v"(wr[k][2]), "+v"(wr[k][3]) :: "memory");
}
template <int NKB, int RWN, int OFF> struct Sch {
  static constexpr int NL = NKB - RWN;
  static constexpr int blk(int i) { return i < NL ? RWN + i : i - NL; }
  static constexpr int slot(int i) { return (RWN + i + OFF) % WL; }
  static constexpr bool dma(int s) { return s >= 1 && s - 1 + WL < NL; }
  static constexpr int younger(int i) {
    int pos = 0, pa = -1, pw = -1;
    for (int k = 0; k < AD; ++k) { if (k == i) pa = pos; ++pos; }
    for (int s = 0; s < i; ++s) {
      if (dma(s)) { if (s - 1 + WL == i + 1) pw = pos; ++pos; }
      if (s + AD < NKB) { if (s + AD == i) pa = pos; ++pos; }
    }
    const int need = pa > pw ? pa : pw;
    return pos - 1 - need;
  }
};
DI bf16x8 tr_frag(const char* p) {
  s16x4 lo = __builtin_amdgcn_ds_read_tr16_b64_v4i16((LDSP s16x4*)(p));
  s16x4 hi = __builtin_amdgcn_ds_read_tr16_b64_v4i16((LDSP s16x4*)(p + 512));
  return __builtin_shufflevector(lo, hi, 0, 1, 2, 3, 4, 5, 6, 7);
}
template <int NKB, int RB, bool SWAP, int RWN, int OFF>
DI void gemm_main(const char* ab, const char* abo, const char* wb, const unsigned (&vo)[4], unsigned lring, const TrAddr& tra,
                  bf16x8 (&wr)[RWN][4], int w, int lane, f32x4 (&acc)[4][4], char* park, volatile LDSP unsigned* sig) {
  typedef Sch<NKB, RWN, OFF> S;
  unsigned va[4];
#pragma unroll
  for (int t = 0; t < 4; ++t) va[t] = lane * 16 + ((t + w + 1) & 3) * 1024;
  u32x4 av[AD][4];
  bf16x8 fr[4];
#define ABASE(i) (((S::blk(i) & 1) ? abo : ab) + (size_t)S::blk(i) * 16384)
#define LDA1(i, t) asm volatile("global_load_dwordx4 %0, %1, %2 sc1" : "=&v"(av[(i) % AD][t]) : "v"(va[t]), "s"(ABASE(i)) : "memory")
#define DMA1(i, q) asm volatile("s_mov_b32 m0, %0\n\ts_nop 0\n\tglobal_load_lds_dwordx4 %1, %2 offset:%3" \
                 :: "s"(__builtin_amdgcn_readfirstlane(lring + S::slot((i) - 1) * 4096)), "v"(vo[q]), "s"(wb + (size_t)(RWN + (i) - 1 + WL) * 128 * RB), "i"((q) * 1024) : "memory")
#pragma unroll
  for (int i = 0; i < AD; ++i) { LDA1(i, 0); LDA1(i, 1); LDA1(i, 2); LDA1(i, 3); }
  asm volatile("s_waitcnt vmcnt(%0)" :: "i"(4 * AD) : "memory");
#pragma unroll
  for (int j = 0; j < 4; ++j) fr[j] = tr_frag(tra.p[j] + S::slot(0) * 4096);
#pragma unroll
  for (int i = 0; i < NKB; ++i) {
    const int st = i % AD;
    asm volatile("s_waitcnt vmcnt(%4)" : "+v"(av[st][0]), "+v"(av[st][1]), "+v"(av[st][2]), "+v"(av[st][3]) : "i"(4 * S::younger(i)) : "memory");
    if (i == (SWAP ? PSTEP : GSTEP) && lane == 0) *sig = 1;
    if (i == NKB - 1) {
#pragma unroll
      for (int m = 0; m < 16; ++m) {
        const int t = m >> 2, j = m & 3;
        if (SWAP) asm volatile("v_mfma_f32_16x16x32_bf16 %0, %1, %2, %0" : "+v"(acc[j][t]) : "v"(av[st][t]), "v"(wr[RWN - 1][j]) : "memory");
        else asm volatile("v_mfma_f32_16x16x32_bf16 %0, %1, %2, %0" : "+v"(acc[j][t]) : "v"(wr[RWN - 1][j]), "v"(av[st][t]) : "memory");
        if (m >= 8) *(f32x4*)(park + (m - 8) * 1024) = acc[(m - 8) & 3][(m - 8) >> 2];
      }
      continue;
    }
#pragma unroll
    for (int j = 0; j < 4; ++j) {
#pragma unroll
      for (int t = 0; t < 4; ++t) {
        const bf16x8 wf = i < S::NL ? fr[j] : wr[i < S::NL ? 0 : i - S::NL][j];
        if (i == 0) {
          if (SWAP) asm volatile("v_mfma_f32_16x16x32_bf16 %0, %1, %2, 0" : "=&v"(acc[j][t]) : "v"(av[st][t]), "v"(wf) : "memory");
          else asm volatile("v_mfma_f32_16x16x32_bf16 %0, %1, %2, 0" : "=&v"(acc[j][t]) : "v"(wf), "v"(av[st][t]) : "memory");
        } else {
          if (SWAP) asm volatile("v_mfma_f32_16x16x32_bf16 %0, %1, %2, %0" : "+v"(acc[j][t]) : "v"(av[st][t]), "v"(wf) : "memory");
          else asm volatile("v_mfma_f32_16x16x32_bf16 %0, %1, %2, %0" : "+v"(acc[j][t]) : "v"(wf), "v"(av[st][t]) : "memory");
        }
        if (S::dma(i) && j < 2 && (t == 0 || t == 2)) { if (j == 0 && t == 0) DMA1(i, 0); else if (j == 0) DMA1(i, 1); else if (t == 0) DMA1(i, 2); else DMA1(i, 3); }
        if (j == 3 && i + AD < NKB) { if (t == 0) LDA1(i + AD, 0); else if (t == 1) LDA1(i + AD, 1); else if (t == 2) LDA1(i + AD, 2); else LDA1(i + AD, 3); }
      }
      if (i + 1 < S::NL) fr[j] = tr_frag(tra.p[j] + S::slot(i + 1) * 4096);
    }
  }
#pragma unroll
  for (int j = 0; j < 4; ++j) { *(f32x4*)(park + (8 + j) * 1024) = acc[j][2]; asm volatile("" ::: "memory"); }
  asm volatile("s_nop 7\n\ts_nop 7" ::: "memory");
#undef LDA1
#undef DMA1
#undef ABASE
}
DI void reduce4(f32x4 (&acc)[4][4], f32x4 (&res)[4], char* lds, int w, int lane, volatile LDSP unsigned* words, unsigned ph) {
  grp_bar<0>(words, w, ph, lane);
  f32x4 pv[3][4];
#pragma unroll
  for (int k = 1; k < 4; ++k) {
    const char* p = lds + ((w + k) & 3) * (WL * 4096) + (3 - k) * 4096 + lane * 16;
#pragma unroll
    for (int j = 0; j < 4; ++j) pv[k - 1][j] = *(const f32x4*)(p + j * 1024);
  }
#pragma unroll
  for (int j = 0; j < 4; ++j) res[j] = (acc[j][3] + pv[0][j]) + (pv[1][j] + pv[2][j]);
}

struct EpiIn { u32x2 xv[2], sv[2]; unsigned braw[2]; size_t oo; };
DI void epi_load(EpiIn& e, const u32x4* x, cus b2, int sTl, int dT2, int w, int j0, int lane) {
  const int g = lane >> 4, c = lane & 15;
  e.oo = (size_t)(sTl * 64 + 16 * j0 + c) * D_ + dT2 * 64 + 16 * w + 4 * g;
#pragma unroll
  for (int j = 0; j < 2; ++j) {
    e.braw[j] = b2[sTl * 64 + 16 * (j0 + j) + c];
    e.xv[j] = *(const u32x2*)((cus)x + e.oo + (size_t)(16 * j) * D_);
  }
}
DI void epi_load_s(EpiIn& e, const char* st, int w, int j0, int lane) {
#pragma unroll
  for (int j = 0; j < 2; ++j)
    e.sv[j] = __builtin_bit_cast(u32x2, __builtin_amdgcn_ds_read_tr16_b64_v4i16((LDSP s16x4*)(st + (16 * w + (lane >> 2)) * 144 + (j0 + j) * 32 + (lane & 3) * 8)));
}
DI void epi_finish(const EpiIn& e, f32x4 own0, f32x4 own1, const char* lds, unsigned short* out, int w, int j0, int lane) {
  f32x4 pv[3][2];
#pragma unroll
  for (int k = 1; k < 4; ++k) {
    const char* p = lds + ((w + k) & 3) * (WL * 4096) + (3 - k) * 4096 + j0 * 1024 + lane * 16;
#pragma unroll
    for (int j = 0; j < 2; ++j) pv[k - 1][j] = *(const f32x4*)(p + j * 1024);
  }
#pragma unroll
  for (int j = 0; j < 2; ++j) {
    const f32x4 r = ((j ? own1 : own0) + pv[0][j]) + (pv[1][j] + pv[2][j]) + bf_lo(e.braw[j]);
    const f32x2 o0 = __builtin_elementwise_fma(bf_pair(e.sv[j][0]), silu2(f32x2{r[0], r[1]}), bf_pair(e.xv[j][0]));
    const f32x2 o1 = __builtin_elementwise_fma(bf_pair(e.sv[j][1]), silu2(f32x2{r[2], r[3]}), bf_pair(e.xv[j][1]));
    *(u32x2*)(out + e.oo + (size_t)(16 * j) * D_) = u32x2{pk_bf16(o0[0], o0[1]), pk_bf16(o1[0], o1[1])};
  }
}

struct Args {
  const u32x4* x; cus Are, Aim, Bre, Bim, Cre, Cim, logdt, Dskip, w1, b1, w2, b2;
  unsigned short* out;
  char* Hf;  char* X2; char* Gf; char* sT; unsigned* fl;
};
constexpr int FL_H = 64, FL_G = 1024;

constexpr int KR = 2080;
constexpr int UPAD = 256;
constexpr int R1OFF = KR * 2 + 128;
constexpr int KT_B = R1OFF + KR * 2 + 32;
constexpr int U_B = (UPAD + L_) * 2;
constexpr int XR = 4 * WL * 4096;
constexpr int X_KT = XR;
constexpr int X_U = X_KT + 2 * KT_B;
constexpr int X_SC = X_U + 2 * U_B;
constexpr int SC_B = 8192 + 2 * TABN * 8;
constexpr int X_SY = X_SC + SC_B;
constexpr int X_B1 = X_SY + 128;
constexpr int X_DUM = X_B1 + 512;
constexpr int LDS_B = X_DUM + 256;
static_assert(LDS_B <= 160 * 1024 && SC_B >= 16384 && 2 * KT_B >= 16 * (1024 + 32), "LDS");

__device__ __attribute__((noinline)) void code_end_marker();

__global__ __launch_bounds__(512, 2) void k_mega(Args a) {
  extern __shared__ __attribute__((aligned(16))) char lds[];
  const int tid = threadIdx.x, lane = tid & 63, w = __builtin_amdgcn_readfirstlane(tid >> 6);
  const int b = blockIdx.x, xg = b & 7, r_ = b >> 3;
  const int tb = xg * 32 + r_;
  const unsigned lbase = (unsigned)(size_t)(LDSP char*)lds;
  volatile LDSP unsigned* sy = (volatile LDSP unsigned*)(lds + X_SY);
  const bool has1 = r_ < 28;
  const int idx1 = xg * 28 + (has1 ? r_ : 0);
  const int fT = idx1 >> 3, dT = idx1 & 7;
  const int sTl = tb >> 3, dT2 = tb & 7;
  u32x4 xr; unsigned ep;
  asm volatile("global_load_dwordx4 %0, %2, %3\n\tglobal_load_dword %1, %4, %5 sc1" : "=&v"(xr), "=&v"(ep)
               : "v"(lane * 16), "s"(a.x + (size_t)(tb * 8 + w) * 64), "v"(0), "s"(a.fl) : "memory");
  if (tid < 32) sy[tid] = 0;
  unsigned praw[8];
  if (w >= 4) {
    const int i = (tb * 2) * N_ + (w - 4) * 64;
    const int dch = tb * 2 + ((w - 4) >> 1);
    asm volatile("global_load_ushort %0, %8, %10\n\tglobal_load_ushort %1, %8, %11\n\tglobal_load_ushort %2, %8, %12\n\tglobal_load_ushort %3, %8, %13\n\t"
                 "global_load_ushort %4, %8, %14\n\tglobal_load_ushort %5, %8, %15\n\tglobal_load_ushort %6, %9, %16\n\tglobal_load_ushort %7, %9, %17\n\ts_barrier"
                 : "=&v"(praw[0]), "=&v"(praw[1]), "=&v"(praw[2]), "=&v"(praw[3]), "=&v"(praw[4]), "=&v"(praw[5]), "=&v"(praw[6]), "=&v"(praw[7])
                 : "v"(lane * 2), "v"(0), "s"(a.Are + i), "s"(a.Aim + i), "s"(a.Bre + i), "s"(a.Bim + i), "s"(a.Cre + i), "s"(a.Cim + i),
                   "s"(a.logdt + dch), "s"(a.Dskip + dch) : "memory");
    const int li = r_ * 256 + tid - 256, ft0 = (xg * 28) >> 3;
    const char* t1 = (const char*)a.w1 + (size_t)(li >> 2) * (F_ * 2) + (ft0 + (li & 3)) * 128;
    const char* t2 = t1;
    {
      unsigned long long pc;
      asm volatile("s_getpc_b64 %0" : "=s"(pc));
      const unsigned long long pe = (unsigned long long)(void*)&code_end_marker;
      const unsigned long long pa = (pc & ~127ull) + (unsigned long long)(((w - 4) * 4 + (r_ & 3)) * 64 + lane) * 128;
      if (pe > pc && pe - pc < 65536 && pa + 4 <= pe && r_ >= 28 && w < 6) t2 = (const char*)pa;
    }
    asm volatile("s_mov_b32 m0, %2\n\ts_nop 0\n\tglobal_load_lds_dword %1, off\n\tglobal_load_lds_dword %0, off\n\ts_waitcnt vmcnt(10)"
                 :: "v"(t1), "v"(t2), "s"(lbase + X_DUM) : "memory");
  } else {
    u32x4 bv;
    const u32x4 mark = {~0u, ~0u, ~0u, ~0u};
    asm volatile("global_load_dwordx4 %0, %1, %2\n\ts_barrier\n\tglobal_store_dwordx4 %3, %4, %5 sc1\n\tglobal_store_dwordx4 %3, %4, %5 offset:1024 sc1\n\t"
                 "s_waitcnt vmcnt(2)"
                 : "=&v"(bv) : "v"((lane & 7) * 16), "s"(a.b1 + fT * 64), "v"(lane * 16), "v"(mark), "s"(a.sT + (size_t)tb * 8192 + w * 2048) : "memory");
    if (lane < 8) *(u32x4*)(lds + X_B1 + w * 128 + lane * 16) = bv;
  }
  asm volatile("" : "+v"(xr), "+v"(ep));
  const unsigned tag = __builtin_amdgcn_readfirstlane(ep) + 1u;
  constexpr int RS = 1024 + 32;
  {
    u32x4 o1, o2;
    ln8<true>(xr, o1);
    *(u32x4*)(lds + X_KT + w * RS + lane * 16) = o1;
    ln8<false>(o1, o2);
    *(u32x4*)(lds + X_KT + (8 + w) * RS + lane * 16) = o2;
  }
  lds_barrier();

  if (__builtin_expect(w < 4, 1)) {
    __builtin_amdgcn_s_setprio(3);
    const int g = lane >> 4, c = lane & 15, q = (lane & 15) >> 2, p = lane & 3;
    {
      u32x4 ov[2][2];
#pragma unroll
      for (int arr = 1; arr >= 0; --arr)
#pragma unroll
        for (int it = 0; it < 2; ++it) {
          const char* base = lds + X_KT + (arr * 8 + q) * RS + (w * 8 + it * 4 + g) * 32 + p * 8;
          s16x4 lo = __builtin_amdgcn_ds_read_tr16_b64_v4i16((LDSP s16x4*)(base));
          s16x4 hi = __builtin_amdgcn_ds_read_tr16_b64_v4i16((LDSP s16x4*)(base + 4 * RS));
          ov[arr][it] = __builtin_bit_cast(u32x4, __builtin_shufflevector(lo, hi, 0, 1, 2, 3, 4, 5, 6, 7));
        }
#pragma unroll
      for (int arr = 1; arr >= 0; --arr)
#pragma unroll
        for (int it = 0; it < 2; ++it) {
          const int dt = w * 8 + it * 4 + g;
          if (arr) st16_dev(a.Hf + ((size_t)((dt >> 2) * 64 + kpos(tb >> 2)) * 4 + (dt & 3)) * 1024 + ((tb & 3) * 16 + c) * 16, ov[arr][it]);
          else st16_dev(a.X2 + ((size_t)tb * 512 + dt * 16 + c) * 16, ov[arr][it]);
        }
    }
    const unsigned lring = lbase + w * (WL * 4096);
    TrAddr tra;
    tr_addr(tra, lds + w * (WL * 4096), lane);
    f32x4 acc[4][4], res[4];
    unsigned vo[4];
    w_offsets<F_ * 2>(lane, vo);
    const char* wb1 = (const char*)a.w1 + (size_t)(w * 32) * (F_ * 2) + fT * 128;
    const char* wb2 = (const char*)a.w2 + (size_t)(w * 32) * (L_ * 2) + sTl * 128;
    unsigned long long tH = 0;
    bf16x8 wr2[RW2][4];
    asm volatile("s_waitcnt vmcnt(0)" ::: "memory");
    grp_arrive(sy + 12, w, 1, lane);
    if (w == 0) {
      grp_wait<0>(sy + 12, 1);
      if (lane == 0) { st4_dev(a.fl + FL_H + tb, tag); sy[8] = 1; }
    } else word_wait<0>(sy + 8, 1);
    if (!has1) __builtin_amdgcn_s_setprio(0);
    if (has1) {
      static_assert(GSTEP >= 16 - RW1, "the ring must be drained before it is handed over");
      bf16x8 wr1[RW1][4];
      w_issue<F_ * 2, 0>(wb1, vo, lring, 0, WL);
      asm volatile("s_waitcnt vmcnt(%0)" :: "i"(4 * (WL - RW1)) : "memory");
      w_to_regs<RW1, 0>(tra, wr1);
      w_issue<F_ * 2, 0>(wb1, vo, lring, WL, WL + RW1);
      if (w == 0) {
        poll_flags4(a.fl + FL_H + lane * 4, tag);
        if (lane == 0) sy[11] = 1;
      } else word_wait<1>(sy + 11, 1);
      gemm_main<16, F_ * 2, false, RW1, 0>(a.Hf + (size_t)(dT * 64 + w) * 4096, a.Hf + (size_t)(dT * 64 + (w ^ 2)) * 4096, wb1, vo, lring, tra, wr1, w, lane, acc,
                                           lds + w * (WL * 4096) + lane * 16, sy + 20 + w);
      w_offsets<L_ * 2>(lane, vo);
      reduce4(acc, res, lds, w, lane, sy, 1);
      u32x2 o[4];
#pragma unroll
      for (int j = 0; j < 4; ++j) {
        const u32x2 bb = *(const u32x2*)(lds + X_B1 + w * 128 + (16 * j + 4 * g) * 2);
        const f32x2 v0 = gelu_tanh2(f32x2{res[j][0], res[j][1]} + bf_pair(bb[0])), v1 = gelu_tanh2(f32x2{res[j][2], res[j][3]} + bf_pair(bb[1]));
        o[j] = u32x2{pk_bf16(v0[0], v0[1]), pk_bf16(v1[0], v1[1])};
      }
#pragma unroll
      for (int jp = 0; jp < 2; ++jp) {
        u32x4 q4;
#pragma unroll
        for (int e = 0; e < 2; ++e) {
          auto sw = __builtin_amdgcn_permlane16_swap(o[2 * jp][e], o[2 * jp + 1][e], false, false);
          q4[e] = sw[0]; q4[2 + e] = sw[1];
        }
        const int f = fT * 64 + 16 * (2 * jp + (g & 1)) + 8 * (g >> 1);
        st16_dev(a.Gf + ((size_t)(dT * 56 + kpos(f >> 5)) * 4 + w) * 1024 + (((f & 31) >> 3) * 16 + c) * 16, q4);
      }
      grp_arrive(sy + 24, w, 1, lane);
      asm volatile("s_waitcnt vmcnt(0)" ::: "memory");
      grp_arrive(sy + 12, w, 2, lane);
      if (w == 0) {
        grp_wait<0>(sy + 12, 2);
        if (lane == 0) { st4_dev(a.fl + FL_G + (dT * 32 + fT) * GSP, tag); sy[9] = 1; }
      } else word_wait<0>(sy + 9, 1);
      w_issue<L_ * 2, 3>(wb2, vo, lring, XS2, WL);
      word_wait<0>(sy + 28 + w, 1);
      w_to_regs<RW2, 3, 0, XS2>(tra, wr2);
    } else {
      const int bq = r_ - 28;
      tH = __builtin_amdgcn_s_memrealtime();
#pragma unroll 1
      for (int li = tid; li < 1792; li += 256) {
        touch(lbase + X_DUM, (const char*)a.w2 + (size_t)((li >> 2) * 4 + bq) * (L_ * 2) + (xg * 4 + (li & 3)) * 128);
        asm volatile("s_waitcnt vmcnt(%0)" :: "i"(3) : "memory");
      }
      asm volatile("s_waitcnt vmcnt(0)" ::: "memory");
      grp_bar<2>(sy + 12, w, 2, lane);
      if (w == 0) {
        poll_flags4(a.fl + FL_H + lane * 4, tag);
        if (lane == 0) sy[11] = 1;
      }
      grp_wait<2>(sy + 4, 3);
      w_offsets<L_ * 2>(lane, vo);
      w_issue<L_ * 2, 3>(wb2, vo, lring, 0, WL);
      asm volatile("s_waitcnt vmcnt(0)" ::: "memory");
      w_to_regs<RW2, 3, 0, XS2>(tra, wr2);
    }
    static_assert(XS2 <= WL - 3 && RW2 >= XS2 && RW2 <= WL, "");
    w_issue<L_ * 2, 3>(wb2, vo, lring, WL, XS2 + WL);
    if (RW2 > XS2) {
      asm volatile("s_waitcnt vmcnt(%0)" :: "i"(4 * (XS2 + WL - RW2)) : "memory");
      w_to_regs<RW2, 3, XS2, RW2>(tra, wr2);
      w_issue<L_ * 2, 3>(wb2, vo, lring, XS2 + WL, RW2 + WL);
    }
    if (w == 0) {
      if (!has1) {
#pragma unroll 1
        for (int sp = 0; sp < 4096 && __builtin_amdgcn_s_memrealtime() - tH < IDEL; ++sp) __builtin_amdgcn_s_sleep(4);
      }
      poll_flags<PBK>(a.fl + FL_G + (dT2 * 32 + (lane & 31)) * GSP, tag, lane >= 28);
      if (lane == 0) sy[11] = 2;
    } else word_wait<1>(sy + 11, 2);
    __builtin_amdgcn_s_setprio(3);
    gemm_main<14, L_ * 2, true, RW2, 3>(a.Gf + (size_t)(dT2 * 56 + w) * 4096, a.Gf + (size_t)(dT2 * 56 + (w ^ 2)) * 4096, wb2, vo, lring, tra, wr2, w, lane, acc,
                                        lds + w * (WL * 4096) + lane * 16, sy + 19);
    *(f32x4*)(lds + w * (WL * 4096) + 3 * 4096 + lane * 16) = acc[2][3];
    *(f32x4*)(lds + w * (WL * 4096) + 3 * 4096 + 1024 + lane * 16) = acc[3][3];
    grp_arrive(sy, w, 2, lane);
    EpiIn ei;
    epi_load(ei, a.x, a.b2, sTl, dT2, w, 0, lane);
    word_wait<0>(sy + 10, 1);
    epi_load_s(ei, lds + X_SC, w, 0, lane);
    grp_wait<0>(sy, 2);
    epi_finish(ei, acc[0][3], acc[1][3], lds, a.out, w, 0, lane);
  } else {
    const int j4 = w - 4;
    volatile LDSP unsigned* xb = sy + 4;
    asm volatile("s_waitcnt vmcnt(2)" : "+v"(praw[0]), "+v"(praw[1]), "+v"(praw[2]), "+v"(praw[3]), "+v"(praw[4]), "+v"(praw[5]), "+v"(praw[6]), "+v"(praw[7]) :: "memory");
    const int ch = j4 >> 1, hw = j4 & 1;
    const int d = tb * 2 + ch;
    {
      float* par = (float*)(lds + X_SC + j4 * 2048);
      float* tabf = (float*)(lds + X_SC + 8192 + ch * (TABN * 8));
      const float dt = rbf(__expf(bf_lo(praw[6])));
      const float zr = dt * bf_lo(praw[0]) * LOG2E, zi = dt * bf_lo(praw[1]) * 0.15915494309189535f;
      const float br = dt * bf_lo(praw[2]), bi = dt * bf_lo(praw[3]), cr = bf_lo(praw[4]), ci = bf_lo(praw[5]);
      float wr = cr * br - ci * bi, wi = cr * bi + ci * br;
      const float zmax = wave_max_nn(fmaxf(zr, 0.f));
      const float wmax = wave_max_nn(fmaxf(fabsf(wr), fabsf(wi)));
      const unsigned we = (__float_as_uint(wmax) >> 23) & 0xff;
      const unsigned wec = we < 16 ? 16 : (we > 250 ? 250 : we);
      const float wsc = __uint_as_float((266u - wec) << 23);
      const float osc = __uint_as_float((wec - 12u) << 23) * 6.103515625e-05f;
      const float zs = zr - zmax;
      wr *= wsc; wi *= wsc;
      float qr, qi, er, ei;
      cexp2(zs * 128.f, zi * 128.f, qr, qi);
      cexp2(zs * 1024.f, zi * 1024.f, er, ei);
      *(f32x4*)(par + lane * 8) = f32x4{zs, zi, wr, wi};
      *(f32x4*)(par + lane * 8 + 4) = f32x4{qr, qi, er, ei};
      const float p2r = er * er - ei * ei, p2i = 2.f * er * ei;
      const float k2048 = wave_sum(wr * p2r - wi * p2i) * 16384.f;
      const int r = lane & 15, g = lane >> 4;
      const float va = 8.f * r;
      const int off = r < 8 ? r : r < 12 ? 256 + 2 * (r - 8) : 512 + 256 * ((r - 12) >> 1) + 4 * (r & 1);
      const float vb = (float)off;
      f32x4 acc[2][2];
#pragma unroll
      for (int t = 0; t < 4; ++t) acc[t >> 1][t & 1] = f32x4{0.f, 0.f, 0.f, 0.f};
#pragma unroll 1
      for (int ks = 0; ks < 4; ++ks) {
        u32x4 a0, a1, b0, b1;
#pragma unroll
        for (int j = 0; j < 4; ++j) {
          const int n = ks * 16 + g * 4 + j;
          const f32x4 pp = *(const f32x4*)(par + n * 8), pq = *(const f32x4*)(par + n * 8 + 4);
          float tr, ti, xr_, xi_;
          cexp2(__builtin_fmaf(pp[0], va, 14.f), pp[1] * va, tr, ti);
          cexp2(pp[0] * vb, pp[1] * vb, xr_, xi_);
          const float ur = pp[2] * xr_ - pp[3] * xi_, ui = pp[2] * xi_ + pp[3] * xr_;
          a0[j] = pk_f16(tr, ti);
          a1[j] = pk_f16(tr * pq[0] - ti * pq[1], tr * pq[1] + ti * pq[0]);
          b0[j] = pk_f16(ur, -ui);
          b1[j] = pk_f16(ur * pq[2] - ui * pq[3], -(ur * pq[3] + ui * pq[2]));
        }
        acc[0][0] = __builtin_amdgcn_mfma_f32_16x16x32_f16(__builtin_bit_cast(f16x8, a0), __builtin_bit_cast(f16x8, b0), acc[0][0], 0, 0, 0);
        acc[1][0] = __builtin_amdgcn_mfma_f32_16x16x32_f16(__builtin_bit_cast(f16x8, a1), __builtin_bit_cast(f16x8, b0), acc[1][0], 0, 0, 0);
        acc[0][1] = __builtin_amdgcn_mfma_f32_16x16x32_f16(__builtin_bit_cast(f16x8, a0), __builtin_bit_cast(f16x8, b1), acc[0][1], 0, 0, 0);
        acc[1][1] = __builtin_amdgcn_mfma_f32_16x16x32_f16(__builtin_bit_cast(f16x8, a1), __builtin_bit_cast(f16x8, b1), acc[1][1], 0, 0, 0);
      }
      float kl = k2048 * osc;
#pragma unroll
      for (int t = 0; t < 4; ++t) acc[t >> 1][t & 1] *= osc;
      if (zmax > 0.f) {
#pragma unroll
        for (int t = 0; t < 4; ++t)
#pragma unroll
          for (int e = 0; e < 4; ++e)
            acc[t >> 1][t & 1][e] *= __builtin_amdgcn_exp2f(zmax * (vb + (float)(8 * (16 * (t >> 1) + e) + 1024 * (t & 1)) + 32.f * g));
        kl *= __builtin_amdgcn_exp2f(zmax * 2048.f);
      }
      {
        const int stride = r < 8 ? 8 : r < 12 ? 4 : 2;
        const int base = r < 8 ? r : r < 12 ? 256 + (r - 8) : 384 + 64 * ((r - 12) >> 1) + (r & 1);
        float* p0 = tabf + (base + stride * 4 * g) * 2 + hw;
#pragma unroll
        for (int it = 0; it < 2; ++it)
#pragma unroll
          for (int e = 0; e < 4; ++e) p0[stride * (16 * it + e) * 2] = acc[it][0][e];
        if ((off & 255) == 0) {
          float* p1 = tabf + (512 + (off >> 3) + 4 * g) * 2 + hw;
#pragma unroll
          for (int it = 0; it < 2; ++it)
#pragma unroll
            for (int e = 0; e < 4; ++e) p1[(16 * it + e) * 2] = acc[it][1][e];
        }
        if (lane < 2) tabf[(640 + lane) * 2 + hw] = lane ? 0.f : kl;
      }
      grp_arrive(xb, j4, 1, lane);
      const int l2 = hw * 64 + lane;
      char* lk = lds + X_KT + ch * KT_B;
      int ix[17];
#pragma unroll
      for (int e = 0; e < 17; e += 2) {
        const int j0 = 2047 - 16 * l2 - e, j1 = j0 - 1;
        const unsigned bb = pk_bf16((float)j0, (float)j1);
        const int i0 = (int)(bb & 0xffff) - (0x4300 - 128), i1 = (int)(bb >> 16) - (0x4300 - 128);
        ix[e] = j0 < 128 ? (j0 < 0 ? 641 : j0) : i0;
        if (e + 1 < 17) ix[e + 1] = j1 < 128 ? j1 : i1;
      }
      word_wait<1>(sy + 8, 1);
      if (l2 < 8) {
        const int pc = (l2 & 2) ? 258 + (l2 & 1) : (l2 & 1);
        *(u32x4*)(lk + (l2 >> 2) * R1OFF + pc * 16) = u32x4{0, 0, 0, 0};
      }
      if (j4 == 0) *(u32x4*)(lds + X_U + (lane & 1) * U_B + (lane >> 1) * 16) = u32x4{0, 0, 0, 0};
#pragma unroll
      for (int e = 0; e < 17; ++e) asm volatile("" : "+v"(ix[e]));
      grp_wait<0>(xb, 1);
      f32x2 tv[17];
#pragma unroll
      for (int e = 0; e < 17; ++e) tv[e] = *(const f32x2*)(tabf + ix[e] * 2);
      __builtin_amdgcn_sched_barrier(0);
      float val[17];
#pragma unroll
      for (int e = 0; e < 17; ++e) val[e] = tv[e][0] + tv[e][1];
#pragma unroll
      for (int hf = 0; hf < 2; ++hf) {
        u32x4 o0, o1;
#pragma unroll
        for (int e = 0; e < 4; ++e) { o0[e] = pk_bf16(val[8 * hf + 2 * e], val[8 * hf + 2 * e + 1]); o1[e] = pk_bf16(val[8 * hf + 2 * e + 1], val[8 * hf + 2 * e + 2]); }
        *(u32x4*)(lk + 32 + l2 * 32 + hf * 16) = o0;
        *(u32x4*)(lk + R1OFF + 32 + l2 * 32 + hf * 16) = o1;
      }
      if (l2 == 0) *(unsigned*)(lk + R1OFF + 28) = pk_bf16(0.f, val[0]);
    }
    {
      const float dsk = bf_lo(praw[7]);
      const char* lk = lds + X_KT + ch * KT_B;
      const char* lu = lds + X_U + ch * U_B;
      const int col = lane & 15, g = lane >> 4;
      const int p00 = 2047 - col + 8 * g;
      const char* gbase = lk + ((p00 & 1) ? R1OFF : 0) + (p00 >> 1) * 4 - 256 * hw;
      const char* bbase = lu + (UPAD - 16 + 16 * col + 8 * g) * 2 - 256 * hw;
      grp_bar<1>(xb, j4, 2, lane);
      bf16x8 gf[4][8], bf[2][8];
#pragma unroll
      for (int rr = 0; rr < 4; ++rr)
#pragma unroll
        for (int i = 0; i < 8; ++i) {
          const unsigned* gp = (const unsigned*)(gbase - 64 * (8 * i + rr));
          u32x4 gv = {gp[0], gp[1], gp[2], gp[3]};
          gf[rr][i] = __builtin_bit_cast(bf16x8, gv);
        }
#pragma unroll
      for (int rr = 0; rr < 4; ++rr)
        asm volatile("" : "+v"(gf[rr][0]), "+v"(gf[rr][1]), "+v"(gf[rr][2]), "+v"(gf[rr][3]), "+v"(gf[rr][4]), "+v"(gf[rr][5]), "+v"(gf[rr][6]), "+v"(gf[rr][7]));
      unsigned vo2[4];
      w_offsets<L_ * 2>(lane, vo2);
      word_wait(sy + 11, 1);
      if (has1) grp_wait<1>(sy + 20, 1);
      {
        u32x4 u0, u1;
        const int t0 = j4 * 64 + (lane >> 1);
        asm volatile("global_load_dwordx4 %0, %2, off sc1\n\tglobal_load_dwordx4 %1, %3, off sc1\n\ts_waitcnt vmcnt(0)" : "=&v"(u0), "=&v"(u1)
                     : "v"(a.X2 + ((size_t)t0 * 512 + tb * 2 + (lane & 1)) * 16), "v"(a.X2 + ((size_t)(t0 + 32) * 512 + tb * 2 + (lane & 1)) * 16) : "memory");
        char* lw = lds + X_U + (lane & 1) * U_B;
        *(u32x4*)(lw + UPAD * 2 + t0 * 16) = u0;
        *(u32x4*)(lw + UPAD * 2 + (t0 + 32) * 16) = u1;
      }
      grp_arrive(xb, j4, 3, lane);
      if (has1) w_issue<L_ * 2, 3>((const char*)a.w2 + (size_t)(j4 * 32) * (L_ * 2) + sTl * 128, vo2, lbase + j4 * (WL * 4096), 0, XS2);
      if (b == 0 && tid == 256) st4_dev(a.fl, tag);
      grp_wait<0>(xb, 3);
#define BLD(set, rr) do { _Pragma("unroll") for (int i = 0; i < 8; ++i) bf[set][i] = *(const bf16x8*)(bbase + 64 * (8 * i - (rr))); } while (0)
      BLD(0, 0);
      BLD(1, 1);
      f32x4 acc[8];
#pragma unroll
      for (int i = 0; i < 4; ++i) {
        const u32x2 uu = *(const u32x2*)(lu + (UPAD + 256 * (2 * i + hw) + 16 * col + 4 * g) * 2);
        const float d0 = hw ? 0.f : dsk, d1 = hw ? dsk : 0.f;
        const f32x4 uf = {bf_lo(uu[0]), bf_hi(uu[0]), bf_lo(uu[1]), bf_hi(uu[1])};
        if (hw) { acc[2 * i] = f32x4{0.f, 0.f, 0.f, 0.f}; acc[2 * i + 1] = uf * d1; } else { acc[2 * i] = uf * d0; acc[2 * i + 1] = f32x4{0.f, 0.f, 0.f, 0.f}; }
      }
      if (has1) {
        asm volatile("s_waitcnt vmcnt(0)" ::: "memory");
        if (lane == 0) sy[28 + j4] = 1;
        grp_wait<1>(sy + 24, 1);
      }
#pragma unroll
      for (int rr = 0; rr < 4; ++rr) {
#pragma unroll
        for (int bq = 0; bq < 8; ++bq)
#pragma unroll
          for (int i = 0; i + bq < 8; ++i) acc[i + bq] = __builtin_amdgcn_mfma_f32_16x16x32_bf16(gf[rr][bq], bf[rr & 1][i], acc[i + bq], 0, 0, 0);
        asm volatile("" : "+v"(acc[0]), "+v"(acc[1]), "+v"(acc[2]), "+v"(acc[3]), "+v"(acc[4]), "+v"(acc[5]), "+v"(acc[6]), "+v"(acc[7]) :: "memory");
        if (rr + 2 < 4) BLD(rr & 1, rr + 2);
        __builtin_amdgcn_sched_barrier(0);
      }
#undef BLD
      __builtin_amdgcn_s_setprio(3);
      char* xc = lds + X_SC + ch * 8192 + lane * 16;
      if (hw) {
#pragma unroll
        for (int i = 0; i < 4; ++i) { *(f32x4*)(xc + 4096 + i * 1024) = acc[2 * i]; acc[2 * i] = acc[2 * i + 1]; }
      } else {
#pragma unroll
        for (int i = 0; i < 4; ++i) *(f32x4*)(xc + i * 1024) = acc[2 * i + 1];
      }
      grp_arrive(xb, j4, 4, lane);
      word_wait<0>(xb + (j4 ^ 1), 4);
      u32x2 o[4];
#pragma unroll
      for (int i = 0; i < 4; ++i) {
        const f32x4 y = acc[2 * i] + *(const f32x4*)(xc + (hw ^ 1) * 4096 + i * 1024);
        o[i] = u32x2{pk_bf16(y[0], y[1]), pk_bf16(y[2], y[3])};
      }
#pragma unroll
      for (int ip = 0; ip < 2; ++ip) {
        u32x4 q4;
#pragma unroll
        for (int e = 0; e < 2; ++e) {
          auto sw = __builtin_amdgcn_permlane16_swap(o[2 * ip][e], o[2 * ip + 1][e], false, false);
          q4[e] = sw[0]; q4[2 + e] = sw[1];
        }
#pragma unroll
        for (int e = 0; e < 4; ++e) q4[e] = min(q4[e], 0xfffffffeu);
        const int t = 256 * (2 * (2 * ip + (g & 1)) + hw) + 16 * col + 8 * (g >> 1);
        st16_dev(a.sT + ((size_t)d * L_ + t) * 2, q4);
      }
      grp_arrive(xb, j4, 5, lane);
    }
    {
      __builtin_amdgcn_s_setprio(0);
      word_wait<2>(sy + 19, 1);
      grp_wait<1>(xb, 5);
      __builtin_amdgcn_s_setprio(3);
      u32x4 s0, s1;
      const int rw_ = j4 * 16 + (lane >> 3);
      const char* sp = a.sT + ((size_t)(dT2 * 64 + rw_) * L_ + sTl * 64) * 2 + (lane & 7) * 16;
#pragma unroll 1
      for (int att = 0; att < 4096; ++att) {
        asm volatile("global_load_dwordx4 %0, %2, off sc1\n\tglobal_load_dwordx4 %1, %3, off sc1\n\ts_waitcnt vmcnt(0)" : "=&v"(s0), "=&v"(s1)
                     : "v"(sp), "v"(sp + (size_t)8 * L_ * 2) : "memory");
        const unsigned mx = max(max(max(s0[0], s0[1]), max(s0[2], s0[3])), max(max(s1[0], s1[1]), max(s1[2], s1[3])));
        if (!__any(mx == ~0u)) break;
        __builtin_amdgcn_s_sleep(8);
      }
      *(u32x4*)(lds + X_SC + rw_ * 144 + (lane & 7) * 16) = s0;
      *(u32x4*)(lds + X_SC + (rw_ + 8) * 144 + (lane & 7) * 16) = s1;
      grp_bar(xb, j4, 6, lane);
      if (tid == 256) sy[10] = 1;
      EpiIn ei;
      epi_load(ei, a.x, a.b2, sTl, dT2, j4, 2, lane);
      epi_load_s(ei, lds + X_SC, j4, 2, lane);
      asm volatile("s_waitcnt vmcnt(0) lgkmcnt(0)" ::: "memory");
      __builtin_amdgcn_s_setprio(0);
      grp_wait<1>(sy, 2);
      __builtin_amdgcn_s_setprio(3);
      const char* ow = lds + j4 * (WL * 4096) + 3 * 4096 + lane * 16;
      epi_finish(ei, *(const f32x4*)ow, *(const f32x4*)(ow + 1024), lds, a.out, j4, 2, lane);
    }
  }
}

__device__ __attribute__((noinline)) void code_end_marker() { asm volatile("s_nop 0"); }

extern "C" void kernel_launch(void* const* d_in, const int* in_sizes, int n_in, void* d_out, int out_size, void* d_ws,
                              size_t ws_size, hipStream_t stream) {
  char* ws = (char*)d_ws;
  Args a;
  a.x = (const u32x4*)d_in[0];
  a.Are = (cus)d_in[1]; a.Aim = (cus)d_in[2]; a.Bre = (cus)d_in[3]; a.Bim = (cus)d_in[4]; a.Cre = (cus)d_in[5]; a.Cim = (cus)d_in[6];
  a.logdt = (cus)d_in[7]; a.Dskip = (cus)d_in[8];
  a.w1 = (cus)d_in[10]; a.b1 = (cus)d_in[11]; a.w2 = (cus)d_in[12]; a.b2 = (cus)d_in[13];
  a.out = (unsigned short*)d_out;
  a.X2 = ws; a.Hf = ws + (2u << 20); a.Gf = ws + (4u << 20); a.sT = ws + (6u << 20); a.fl = (unsigned*)(ws + (8u << 20));
  (void)hipFuncSetAttribute((const void*)k_mega, hipFuncAttributeMaxDynamicSharedMemorySize, LDS_B);
  k_mega<<<256, 512, LDS_B, stream>>>(a);
}
```

```cpp
#include <hip/hip_runtime.h>
#include <hip/hip_bf16.h>
#include <stdint.h>

typedef short s16x4 __attribute__((ext_vector_type(4)));
typedef short bf16x8 __attribute__((ext_vector_type(8)));
typedef _Float16 f16x8 __attribute__((ext_vector_type(8)));
typedef _Float16 h2_t __attribute__((ext_vector_type(2)));
typedef __bf16 bf2_t __attribute__((ext_vector_type(2)));
typedef float f32x2 __attribute__((ext_vector_type(2)));
typedef float f32x4 __attribute__((ext_vector_type(4)));
typedef unsigned u32x2 __attribute__((ext_vector_type(2)));
typedef unsigned u32x4 __attribute__((ext_vector_type(4)));
typedef const unsigned short* cus;
#define DI __device__ __forceinline__
#define LDSP __attribute__((address_space(3)))


#define RW1 5
#define RW2 5
#define WL 7
#define AD 3
#define XS2 4
#define PBK 6
#define HQ 16
#define GSP 32
#define GSTEP (16 - AD)
#define PSTEP 6
#define IDEL 800

constexpr int L_ = 2048, D_ = 512, N_ = 128, F_ = 1792;
constexpr int TABN = 648;
constexpr float LOG2E = 1.44269504088896341f;
constexpr int SPIN_CAP = 1 << 18;

DI unsigned pk_bf16(float lo, float hi) {
  f32x2 f = {lo, hi};
  return __builtin_bit_cast(unsigned, __builtin_convertvector(f, bf2_t));
}
DI unsigned pk_f16(float lo, float hi) {
  f32x2 f = {lo, hi};
  return __builtin_bit_cast(unsigned, __builtin_convertvector(f, h2_t));
}
DI float bf_lo(unsigned u) { return __uint_as_float(u << 16); }
DI float bf_hi(unsigned u) { return __uint_as_float(u & 0xffff0000u); }
DI float rbf(float x) { return bf_lo(pk_bf16(x, 0.f)); }

template <int CTRL, int RM, bool BC> DI float dppf(float v) {
  return __builtin_bit_cast(float, __builtin_amdgcn_update_dpp(0, __builtin_bit_cast(int, v), CTRL, RM, 0xf, BC));
}
DI float wave_sum(float v) {
  v += dppf<0x111, 0xf, true>(v);
  v += dppf<0x112, 0xf, true>(v);
  v += dppf<0x114, 0xf, true>(v);
  v += dppf<0x118, 0xf, true>(v);
  v += dppf<0x142, 0xa, false>(v);
  v += dppf<0x143, 0xc, false>(v);
  return __builtin_bit_cast(float, __builtin_amdgcn_readlane(__builtin_bit_cast(int, v), 63));
}
DI float wave_max_nn(float v) {
  v = fmaxf(v, dppf<0x111, 0xf, true>(v));
  v = fmaxf(v, dppf<0x112, 0xf, true>(v));
  v = fmaxf(v, dppf<0x114, 0xf, true>(v));
  v = fmaxf(v, dppf<0x118, 0xf, true>(v));
  v = fmaxf(v, dppf<0x142, 0xa, false>(v));
  v = fmaxf(v, dppf<0x143, 0xc, false>(v));
  return __builtin_bit_cast(float, __builtin_amdgcn_readlane(__builtin_bit_cast(int, v), 63));
}
DI void touch(unsigned ldum, const void* p) { asm volatile("s_mov_b32 m0, %0\n\ts_nop 0\n\tglobal_load_lds_dword %1, off" :: "s"(ldum), "v"(p) : "memory"); }
DI void lds_barrier() { asm volatile("s_waitcnt lgkmcnt(0)" ::: "memory"); __builtin_amdgcn_s_barrier(); asm volatile("" ::: "memory"); }
DI void st16_dev(void* p, u32x4 v) { asm volatile("global_store_dwordx4 %0, %1, off sc1\n\ts_nop 1" :: "v"(p), "v"(v) : "memory"); }
DI void st4_dev(void* p, unsigned v) { asm volatile("global_store_dword %0, %1, off sc1" :: "v"(p), "v"(v) : "memory"); }
DI unsigned ld4_dev_wait(const void* p) { unsigned r; asm volatile("global_load_dword %0, %1, off sc1\n\ts_waitcnt vmcnt(0)" : "=&v"(r) : "v"(p) : "memory"); return r; }

DI void grp_arrive(volatile LDSP unsigned* words, int idx, unsigned ph, int lane) {
  asm volatile("s_waitcnt lgkmcnt(0)" ::: "memory");
  if (lane == 0) words[idx] = ph;
}
template <int SL = 1>
DI void grp_wait(volatile LDSP unsigned* words, unsigned ph) {
#pragma unroll 1
  for (int sp = 0; sp < SPIN_CAP * 8; ++sp) {
    const u32x4 v = *(volatile LDSP u32x4*)words;
    const unsigned m = min(min(v[0], v[1]), min(v[2], v[3]));
    if (__builtin_amdgcn_readfirstlane(m) >= ph) break;
    if (SL) __builtin_amdgcn_s_sleep(SL);
  }
  asm volatile("" ::: "memory");
}
template <int SL = 1>
DI void grp_bar(volatile LDSP unsigned* words, int idx, unsigned ph, int lane) { grp_arrive(words, idx, ph, lane); grp_wait<SL>(words, ph); }
template <int SL = 1>
DI void word_wait(volatile LDSP unsigned* word, unsigned v) {
#pragma unroll 1
  for (int sp = 0; sp < SPIN_CAP * 8; ++sp) {
    if (__builtin_amdgcn_readfirstlane(*word) >= v) break;
    if (SL) __builtin_amdgcn_s_sleep(SL);
  }
  asm volatile("" ::: "memory");
}
template <int BK = PBK>
DI void poll_flags(const unsigned* p, unsigned tag, bool done) {
#pragma unroll 1
  for (int sp = 0; sp < SPIN_CAP; ++sp) {
    if (!done) done = ld4_dev_wait(p) == tag;
    if (__all(done)) break;
    __builtin_amdgcn_s_sleep(BK);
  }
}
DI void poll_flags4(const unsigned* p, unsigned tag) {
  bool done = false;
#pragma unroll 1
  for (int sp = 0; sp < SPIN_CAP; ++sp) {
    if (!done) {
      u32x4 f;
      asm volatile("global_load_dwordx4 %0, %1, off sc1\n\ts_waitcnt vmcnt(0)" : "=&v"(f) : "v"(p) : "memory");
      done = f[0] == tag && f[1] == tag && f[2] == tag && f[3] == tag;
    }
    if (__all(done)) break;
    __builtin_amdgcn_s_sleep(PBK);
  }
}

DI int kpos(int kb) { return kb ^ (((kb >> 2) & 1) << 1); }

template <bool PIVOT>
DI void ln8(const u32x4& in, u32x4& o) {
  f32x2 v[4];
#pragma unroll
  for (int i = 0; i < 4; ++i) v[i] = f32x2{bf_lo(in[i]), bf_hi(in[i])};
  const float p = PIVOT ? __builtin_bit_cast(float, __builtin_amdgcn_readfirstlane(__builtin_bit_cast(int, v[0][0]))) : 0.f;
  f32x2 t[4];
#pragma unroll
  for (int i = 0; i < 4; ++i) t[i] = PIVOT ? v[i] - p : v[i];
  const f32x2 s2 = (t[0] + t[1]) + (t[2] + t[3]);
  const f32x2 q2 = __builtin_elementwise_fma(t[0], t[0], t[1] * t[1]) + __builtin_elementwise_fma(t[2], t[2], t[3] * t[3]);
  const float ds = wave_sum(s2[0] + s2[1]) * (1.f / D_), dq = wave_sum(q2[0] + q2[1]) * (1.f / D_);
  const float mean = p + ds, var = fmaxf(__builtin_fmaf(-ds, ds, dq), 0.f);
  const float mb = rbf(mean), vb = rbf(var);
  const float den = rbf(__builtin_amdgcn_sqrtf(rbf(vb + 1e-5f)));
  const float inv = __builtin_amdgcn_rcpf(den);
#pragma unroll
  for (int i = 0; i < 4; ++i) {
    const f32x2 c = v[i] - mb;
    const unsigned r = pk_bf16(c[0], c[1]);
    const f32x2 n = f32x2{bf_lo(r), bf_hi(r)} * inv;
    o[i] = pk_bf16(n[0], n[1]);
  }
}
DI void cexp2(float le, float ph, float& re, float& im) {
  const float e = __builtin_amdgcn_exp2f(le);
  const float p = __builtin_amdgcn_fractf(ph);
  re = e * __builtin_amdgcn_cosf(p);
  im = e * __builtin_amdgcn_sinf(p);
}
DI f32x2 xsig2(f32x2 x, f32x2 e) {
  const f32x2 d = f32x2{__builtin_amdgcn_exp2f(e[0]), __builtin_amdgcn_exp2f(e[1])} + 1.f;
  return x * f32x2{__builtin_amdgcn_rcpf(d[0]), __builtin_amdgcn_rcpf(d[1])};
}
DI f32x2 gelu_tanh2(f32x2 x) {
  constexpr float k1 = -2.f * 0.7978845608028654f * LOG2E, k2 = k1 * 0.044715f;
  return xsig2(x, x * __builtin_elementwise_fma(x * x, f32x2{k2, k2}, f32x2{k1, k1}));
}
DI f32x2 silu2(f32x2 x) { return xsig2(x, x * -LOG2E); }
DI f32x2 bf_pair(unsigned u) { return f32x2{bf_lo(u), bf_hi(u)}; }

#define DMA4(ldsaddr, vo, sb) do { unsigned keep_; \
    asm volatile("s_mov_b32 %0, m0\n\ts_mov_b32 m0, %1\n\ts_nop 0\n\t" \
                 "global_load_lds_dwordx4 %2, %6\n\tglobal_load_lds_dwordx4 %3, %6 offset:1024\n\t" \
                 "global_load_lds_dwordx4 %4, %6 offset:2048\n\tglobal_load_lds_dwordx4 %5, %6 offset:3072\n\ts_mov_b32 m0, %0" \
                 : "=&s"(keep_) : "s"(__builtin_amdgcn_readfirstlane(ldsaddr)), "v"(vo[0]), "v"(vo[1]), "v"(vo[2]), "v"(vo[3]), "s"(sb) : "memory"); } while (0)
template <int RB> DI void w_offsets(int lane, unsigned (&vo)[4]) {
  const int lr = lane >> 3, p8 = lane & 7;
#pragma unroll
  for (int j = 0; j < 4; ++j) vo[j] = (8 * j + lr) * RB + ((p8 ^ ((lr & 2) | ((j & 1) << 2))) << 4) - 1024 * j;
}
struct TrAddr { const char* p[4]; };
DI void tr_addr(TrAddr& t, const char* ring, int lane) {
  const int g = lane >> 4, q = (lane & 15) >> 2, p = lane & 3;
  const int frow = (q & 2) | ((g & 1) << 2);
  const char* trb = ring + (8 * g + q) * 128 + (p & 1) * 8;
#pragma unroll
  for (int j = 0; j < 4; ++j) t.p[j] = trb + (((2 * j + (p >> 1)) ^ frow) << 4);
}
DI void tr_read(bf16x8 (&fr)[4], const TrAddr& t, int off) {
#pragma unroll
  for (int j = 0; j < 4; ++j) {
    s16x4 lo = __builtin_amdgcn_ds_read_tr16_b64_v4i16((LDSP s16x4*)(t.p[j] + off));
    s16x4 hi = __builtin_amdgcn_ds_read_tr16_b64_v4i16((LDSP s16x4*)(t.p[j] + off + 512));
    fr[j] = __builtin_shufflevector(lo, hi, 0, 1, 2, 3, 4, 5, 6, 7);
  }
}
template <int RB, int OFF>
DI void w_issue(const char* wb, const unsigned (&vo)[4], unsigned lring, int k0, int k1) {
#pragma unroll 1
  for (int k = k0; k < k1; ++k) DMA4(lring + ((k + OFF) % WL) * 4096, vo, wb + (size_t)k * 128 * RB);
}
template <int RWN, int OFF, int K0 = 0, int K1 = RWN>
DI void w_to_regs(const TrAddr& tra, bf16x8 (&wr)[RWN][4]) {
#pragma unroll
  for (int k = K0; k < K1; ++k) tr_read(wr[k], tra, ((k + OFF) % WL) * 4096);
#pragma unroll
  for (int k = K0; k < K1; ++k) asm volatile("s_waitcnt lgkmcnt(0)" : "+v"(wr[k][0]), "+v"(wr[k][1]), "+v"(wr[k][2]), "+v"(wr[k][3]) :: "memory");
}
template <int NKB, int RWN, int OFF> struct Sch {
  static constexpr int NL = NKB - RWN;
  static constexpr int blk(int i) { return i < NL ? RWN + i : i - NL; }
  static constexpr int slot(int i) { return (RWN + i + OFF) % WL; }
  static constexpr bool dma(int s) { return s >= 1 && s - 1 + WL < NL; }
  static constexpr int younger(int i) {
    int pos = 0, pa = -1, pw = -1;
    for (int k = 0; k < AD; ++k) { if (k == i) pa = pos; ++pos; }
    for (int s = 0; s < i; ++s) {
      if (dma(s)) { if (s - 1 + WL == i + 1) pw = pos; ++pos; }
      if (s + AD < NKB) { if (s + AD == i) pa = pos; ++pos; }
    }
    const int need = pa > pw ? pa : pw;
    return pos - 1 - need;
  }
};
DI bf16x8 tr_frag(const char* p) {
  s16x4 lo = __builtin_amdgcn_ds_read_tr16_b64_v4i16((LDSP s16x4*)(p));
  s16x4 hi = __builtin_amdgcn_ds_read_tr16_b64_v4i16((LDSP s16x4*)(p + 512));
  return __builtin_shufflevector(lo, hi, 0, 1, 2, 3, 4, 5, 6, 7);
}
template <int NKB, int RB, bool SWAP, int RWN, int OFF>
DI void gemm_main(const char* ab, const char* abo, const char* wb, const unsigned (&vo)[4], unsigned lring, const TrAddr& tra,
                  bf16x8 (&wr)[RWN][4], int w, int lane, f32x4 (&acc)[4][4], char* park, volatile LDSP unsigned* sig) {
  typedef Sch<NKB, RWN, OFF> S;
  unsigned va[4];
#pragma unroll
  for (int t = 0; t < 4; ++t) va[t] = lane * 16 + ((t + w + 1) & 3) * 1024;
  u32x4 av[AD][4];
  bf16x8 fr[4];
#define ABASE(i) (((S::blk(i) & 1) ? abo : ab) + (size_t)S::blk(i) * 16384)
#define LDA1(i, t) asm volatile("global_load_dwordx4 %0, %1, %2 sc1" : "=&v"(av[(i) % AD][t]) : "v"(va[t]), "s"(ABASE(i)) : "memory")
#define DMA1(i, q) asm volatile("s_mov_b32 m0, %0\n\ts_nop 0\n\tglobal_load_lds_dwordx4 %1, %2 offset:%3" \
                 :: "s"(__builtin_amdgcn_readfirstlane(lring + S::slot((i) - 1) * 4096)), "v"(vo[q]), "s"(wb + (size_t)(RWN + (i) - 1 + WL) * 128 * RB), "i"((q) * 1024) : "memory")
#pragma unroll
  for (int i = 0; i < AD; ++i) { LDA1(i, 0); LDA1(i, 1); LDA1(i, 2); LDA1(i, 3); }
  asm volatile("s_waitcnt vmcnt(%0)" :: "i"(4 * AD) : "memory");
#pragma unroll
  for (int j = 0; j < 4; ++j) fr[j] = tr_frag(tra.p[j] + S::slot(0) * 4096);
#pragma unroll
  for (int i = 0; i < NKB; ++i) {
    const int st = i % AD;
    asm volatile("s_waitcnt vmcnt(%4)" : "+v"(av[st][0]), "+v"(av[st][1]), "+v"(av[st][2]), "+v"(av[st][3]) : "i"(4 * S::younger(i)) : "memory");
    if (i == (SWAP ? PSTEP : GSTEP) && lane == 0) *sig = 1;
    if (i == NKB - 1) {
#pragma unroll
      for (int m = 0; m < 16; ++m) {
        const int t = m >> 2, j = m & 3;
        if (SWAP) asm volatile("v_mfma_f32_16x16x32_bf16 %0, %1, %2, %0" : "+v"(acc[j][t]) : "v"(av[st][t]), "v"(wr[RWN - 1][j]) : "memory");
        else asm volatile("v_mfma_f32_16x16x32_bf16 %0, %1, %2, %0" : "+v"(acc[j][t]) : "v"(wr[RWN - 1][j]), "v"(av[st][t]) : "memory");
        if (m >= 8) *(f32x4*)(park + (m - 8) * 1024) = acc[(m - 8) & 3][(m - 8) >> 2];
      }
      continue;
    }
#pragma unroll
    for (int j = 0; j < 4; ++j) {
#pragma unroll
      for (int t = 0; t < 4; ++t) {
        const bf16x8 wf = i < S::NL ? fr[j] : wr[i < S::NL ? 0 : i - S::NL][j];
        if (i == 0) {
          if (SWAP) asm volatile("v_mfma_f32_16x16x32_bf16 %0, %1, %2, 0" : "=&v"(acc[j][t]) : "v"(av[st][t]), "v"(wf) : "memory");
          else asm volatile("v_mfma_f32_16x16x32_bf16 %0, %1, %2, 0" : "=&v"(acc[j][t]) : "v"(wf), "v"(av[st][t]) : "memory");
        } else {
          if (SWAP) asm volatile("v_mfma_f32_16x16x32_bf16 %0, %1, %2, %0" : "+v"(acc[j][t]) : "v"(av[st][t]), "v"(wf) : "memory");
          else asm volatile("v_mfma_f32_16x16x32_bf16 %0, %1, %2, %0" : "+v"(acc[j][t]) : "v"(wf), "v"(av[st][t]) : "memory");
        }
        if (S::dma(i) && j < 2 && (t == 0 || t == 2)) { if (j == 0 && t == 0) DMA1(i, 0); else if (j == 0) DMA1(i, 1); else if (t == 0) DMA1(i, 2); else DMA1(i, 3); }
        if (j == 3 && i + AD < NKB) { if (t == 0) LDA1(i + AD, 0); else if (t == 1) LDA1(i + AD, 1); else if (t == 2) LDA1(i + AD, 2); else LDA1(i + AD, 3); }
      }
      if (i + 1 < S::NL) fr[j] = tr_frag(tra.p[j] + S::slot(i + 1) * 4096);
    }
  }
#pragma unroll
  for (int j = 0; j < 4; ++j) { *(f32x4*)(park + (8 + j) * 1024) = acc[j][2]; asm volatile("" ::: "memory"); }
  asm volatile("s_nop 7\n\ts_nop 7" ::: "memory");
#undef LDA1
#undef DMA1
#undef ABASE
}
DI void reduce4(f32x4 (&acc)[4][4], f32x4 (&res)[4], char* lds, int w, int lane, volatile LDSP unsigned* words, unsigned ph) {
  grp_bar<0>(words, w, ph, lane);
  f32x4 pv[3][4];
#pragma unroll
  for (int k = 1; k < 4; ++k) {
    const char* p = lds + ((w + k) & 3) * (WL * 4096) + (3 - k) * 4096 + lane * 16;
#pragma unroll
    for (int j = 0; j < 4; ++j) pv[k - 1][j] = *(const f32x4*)(p + j * 1024);
  }
#pragma unroll
  for (int j = 0; j < 4; ++j) res[j] = (acc[j][3] + pv[0][j]) + (pv[1][j] + pv[2][j]);
}

struct EpiIn { u32x2 xv[2], sv[2]; unsigned braw[2]; size_t oo; };
DI void epi_load(EpiIn& e, const u32x4* x, cus b2, int sTl, int dT2, int w, int j0, int lane) {
  const int g = lane >> 4, c = lane & 15;
  e.oo = (size_t)(sTl * 64 + 16 * j0 + c) * D_ + dT2 * 64 + 16 * w + 4 * g;
#pragma unroll
  for (int j = 0; j < 2; ++j) {
    e.braw[j] = b2[sTl * 64 + 16 * (j0 + j) + c];
    e.xv[j] = *(const u32x2*)((cus)x + e.oo + (size_t)(16 * j) * D_);
  }
}
DI void epi_load_s(EpiIn& e, const char* st, int w, int j0, int lane) {
#pragma unroll
  for (int j = 0; j < 2; ++j)
    e.sv[j] = __builtin_bit_cast(u32x2, __builtin_amdgcn_ds_read_tr16_b64_v4i16((LDSP s16x4*)(st + (16 * w + (lane >> 2)) * 144 + (j0 + j) * 32 + (lane & 3) * 8)));
}
DI void epi_finish(const EpiIn& e, f32x4 own0, f32x4 own1, const char* lds, unsigned short* out, int w, int j0, int lane) {
  f32x4 pv[3][2];
#pragma unroll
  for (int k = 1; k < 4; ++k) {
    const char* p = lds + ((w + k) & 3) * (WL * 4096) + (3 - k) * 4096 + j0 * 1024 + lane * 16;
#pragma unroll
    for (int j = 0; j < 2; ++j) pv[k - 1][j] = *(const f32x4*)(p + j * 1024);
  }
#pragma unroll
  for (int j = 0; j < 2; ++j) {
    const f32x4 r = ((j ? own1 : own0) + pv[0][j]) + (pv[1][j] + pv[2][j]) + bf_lo(e.braw[j]);
    const f32x2 o0 = __builtin_elementwise_fma(bf_pair(e.sv[j][0]), silu2(f32x2{r[0], r[1]}), bf_pair(e.xv[j][0]));
    const f32x2 o1 = __builtin_elementwise_fma(bf_pair(e.sv[j][1]), silu2(f32x2{r[2], r[3]}), bf_pair(e.xv[j][1]));
    *(u32x2*)(out + e.oo + (size_t)(16 * j) * D_) = u32x2{pk_bf16(o0[0], o0[1]), pk_bf16(o1[0], o1[1])};
  }
}

struct Args {
  const u32x4* x; cus Are, Aim, Bre, Bim, Cre, Cim, logdt, Dskip, w1, b1, w2, b2;
  unsigned short* out;
  char* Hf;  char* X2; char* Gf; char* sT; unsigned* fl;
};
constexpr int FL_H = 64, FL_G = 2048;

constexpr int KR = 2080;
constexpr int UPAD = 256;
constexpr int R1OFF = KR * 2 + 128;
constexpr int KT_B = R1OFF + KR * 2 + 32;
constexpr int U_B = (UPAD + L_) * 2;
constexpr int XR = 4 * WL * 4096;
constexpr int X_KT = XR;
constexpr int X_U = X_KT + 2 * KT_B;
constexpr int X_SC = X_U + 2 * U_B;
constexpr int SC_B = 8192 + 2 * TABN * 8;
constexpr int X_SY = X_SC + SC_B;
constexpr int X_B1 = X_SY + 128;
constexpr int X_DUM = X_B1 + 512;
constexpr int LDS_B = X_DUM + 256;
static_assert(LDS_B <= 160 * 1024 && SC_B >= 16384 && 2 * KT_B >= 16 * (1024 + 32), "LDS");

__device__ __attribute__((noinline)) void code_end_marker();

__global__ __launch_bounds__(512, 2) void k_mega(Args a) {
  extern __shared__ __attribute__((aligned(16))) char lds[];
  const int tid = threadIdx.x, lane = tid & 63, w = __builtin_amdgcn_readfirstlane(tid >> 6);
  const int b = blockIdx.x, xg = b & 7, r_ = b >> 3;
  const int tb = xg * 32 + r_;
  const unsigned lbase = (unsigned)(size_t)(LDSP char*)lds;
  volatile LDSP unsigned* sy = (volatile LDSP unsigned*)(lds + X_SY);
  const bool has1 = r_ < 28;
  const int idx1 = xg * 28 + (has1 ? r_ : 0);
  const int fT = idx1 >> 3, dT = idx1 & 7;
  const int sTl = tb >> 3, dT2 = tb & 7;
  u32x4 xr; unsigned ep;
  asm volatile("global_load_dwordx4 %0, %2, %3\n\tglobal_load_dword %1, %4, %5 sc1" : "=&v"(xr), "=&v"(ep)
               : "v"(lane * 16), "s"(a.x + (size_t)(tb * 8 + w) * 64), "v"(0), "s"(a.fl) : "memory");
  if (tid < 32) sy[tid] = 0;
  unsigned praw[8];
  if (w >= 4) {
    const int i = (tb * 2) * N_ + (w - 4) * 64;
    const int dch = tb * 2 + ((w - 4) >> 1);
    asm volatile("global_load_ushort %0, %8, %10\n\tglobal_load_ushort %1, %8, %11\n\tglobal_load_ushort %2, %8, %12\n\tglobal_load_ushort %3, %8, %13\n\t"
                 "global_load_ushort %4, %8, %14\n\tglobal_load_ushort %5, %8, %15\n\tglobal_load_ushort %6, %9, %16\n\tglobal_load_ushort %7, %9, %17\n\ts_barrier"
                 : "=&v"(praw[0]), "=&v"(praw[1]), "=&v"(praw[2]), "=&v"(praw[3]), "=&v"(praw[4]), "=&v"(praw[5]), "=&v"(praw[6]), "=&v"(praw[7])
                 : "v"(lane * 2), "v"(0), "s"(a.Are + i), "s"(a.Aim + i), "s"(a.Bre + i), "s"(a.Bim + i), "s"(a.Cre + i), "s"(a.Cim + i),
                   "s"(a.logdt + dch), "s"(a.Dskip + dch) : "memory");
    const int li = r_ * 256 + tid - 256, ft0 = (xg * 28) >> 3;
    const char* t1 = (const char*)a.w1 + (size_t)(li >> 2) * (F_ * 2) + (ft0 + (li & 3)) * 128;
    const char* t2 = t1;
    {
      unsigned long long pc;
      asm volatile("s_getpc_b64 %0" : "=s"(pc));
      const unsigned long long pe = (unsigned long long)(void*)&code_end_marker;
      const unsigned long long pa = (pc & ~127ull) + (unsigned long long)(((w - 4) * 4 + (r_ & 3)) * 64 + lane) * 128;
      if (pe > pc && pe - pc < 65536 && pa + 4 <= pe && r_ >= 28 && w < 6) t2 = (const char*)pa;
    }
    asm volatile("s_mov_b32 m0, %2\n\ts_nop 0\n\tglobal_load_lds_dword %1, off\n\tglobal_load_lds_dword %0, off\n\ts_waitcnt vmcnt(10)"
                 :: "v"(t1), "v"(t2), "s"(lbase + X_DUM) : "memory");
  } else {
    u32x4 bv;
    const u32x4 mark = {~0u, ~0u, ~0u, ~0u};
    asm volatile("global_load_dwordx4 %0, %1, %2\n\ts_barrier\n\tglobal_store_dwordx4 %3, %4, %5 sc1\n\tglobal_store_dwordx4 %3, %4, %5 offset:1024 sc1\n\t"
                 "s_waitcnt vmcnt(2)"
                 : "=&v"(bv) : "v"((lane & 7) * 16), "s"(a.b1 + fT * 64), "v"(lane * 16), "v"(mark), "s"(a.sT + (size_t)tb * 8192 + w * 2048) : "memory");
    if (lane < 8) *(u32x4*)(lds + X_B1 + w * 128 + lane * 16) = bv;
  }
  asm volatile("" : "+v"(xr), "+v"(ep));
  const unsigned tag = __builtin_amdgcn_readfirstlane(ep) + 1u;
  constexpr int RS = 1024 + 32;
  {
    u32x4 o1, o2;
    ln8<true>(xr, o1);
    *(u32x4*)(lds + X_KT + w * RS + lane * 16) = o1;
    ln8<false>(o1, o2);
    *(u32x4*)(lds + X_KT + (8 + w) * RS + lane * 16) = o2;
  }
  lds_barrier();

  if (__builtin_expect(w < 4, 1)) {
    __builtin_amdgcn_s_setprio(3);
    const int g = lane >> 4, c = lane & 15, q = (lane & 15) >> 2, p = lane & 3;
    {
      u32x4 ov[2][2];
#pragma unroll
      for (int arr = 1; arr >= 0; --arr)
#pragma unroll
        for (int it = 0; it < 2; ++it) {
          const char* base = lds + X_KT + (arr * 8 + q) * RS + (w * 8 + it * 4 + g) * 32 + p * 8;
          s16x4 lo = __builtin_amdgcn_ds_read_tr16_b64_v4i16((LDSP s16x4*)(base));
          s16x4 hi = __builtin_amdgcn_ds_read_tr16_b64_v4i16((LDSP s16x4*)(base + 4 * RS));
          ov[arr][it] = __builtin_bit_cast(u32x4, __builtin_shufflevector(lo, hi, 0, 1, 2, 3, 4, 5, 6, 7));
        }
#pragma unroll
      for (int arr = 1; arr >= 0; --arr)
#pragma unroll
        for (int it = 0; it < 2; ++it) {
          const int dt = w * 8 + it * 4 + g;
          if (arr) st16_dev(a.Hf + ((size_t)((dt >> 2) * 64 + kpos(tb >> 2)) * 4 + (dt & 3)) * 1024 + ((tb & 3) * 16 + c) * 16, ov[arr][it]);
          else st16_dev(a.X2 + ((size_t)tb * 512 + dt * 16 + c) * 16, ov[arr][it]);
        }
    }
    const unsigned lring = lbase + w * (WL * 4096);
    TrAddr tra;
    tr_addr(tra, lds + w * (WL * 4096), lane);
    f32x4 acc[4][4], res[4];
    unsigned vo[4];
    w_offsets<F_ * 2>(lane, vo);
    const char* wb1 = (const char*)a.w1 + (size_t)(w * 32) * (F_ * 2) + fT * 128;
    const char* wb2 = (const char*)a.w2 + (size_t)(w * 32) * (L_ * 2) + sTl * 128;
    unsigned long long tH = 0;
    bf16x8 wr2[RW2][4];
    asm volatile("s_waitcnt vmcnt(0)" ::: "memory");
    grp_arrive(sy + 12, w, 1, lane);
    if (w == 0) {
      grp_wait<0>(sy + 12, 1);
      if (lane == 0) { st4_dev(a.fl + FL_H + (tb >> 2) * HQ + (tb & 3), tag); sy[8] = 1; }
    } else word_wait<0>(sy + 8, 1);
    if (!has1) __builtin_amdgcn_s_setprio(0);
    if (has1) {
      static_assert(GSTEP >= 16 - RW1, "the ring must be drained before it is handed over");
      bf16x8 wr1[RW1][4];
      w_issue<F_ * 2, 0>(wb1, vo, lring, 0, WL);
      asm volatile("s_waitcnt vmcnt(%0)" :: "i"(4 * (WL - RW1)) : "memory");
      w_to_regs<RW1, 0>(tra, wr1);
      w_issue<F_ * 2, 0>(wb1, vo, lring, WL, WL + RW1);
      if (w == 0) {
        poll_flags4(a.fl + FL_H + lane * HQ, tag);
        if (lane == 0) sy[11] = 1;
      } else word_wait<1>(sy + 11, 1);
      gemm_main<16, F_ * 2, false, RW1, 0>(a.Hf + (size_t)(dT * 64 + w) * 4096, a.Hf + (size_t)(dT * 64 + (w ^ 2)) * 4096, wb1, vo, lring, tra, wr1, w, lane, acc,
                                           lds + w * (WL * 4096) + lane * 16, sy + 20 + w);
      w_offsets<L_ * 2>(lane, vo);
      reduce4(acc, res, lds, w, lane, sy, 1);
      u32x2 o[4];
#pragma unroll
      for (int j = 0; j < 4; ++j) {
        const u32x2 bb = *(const u32x2*)(lds + X_B1 + w * 128 + (16 * j + 4 * g) * 2);
        const f32x2 v0 = gelu_tanh2(f32x2{res[j][0], res[j][1]} + bf_pair(bb[0])), v1 = gelu_tanh2(f32x2{res[j][2], res[j][3]} + bf_pair(bb[1]));
        o[j] = u32x2{pk_bf16(v0[0], v0[1]), pk_bf16(v1[0], v1[1])};
      }
#pragma unroll
      for (int jp = 0; jp < 2; ++jp) {
        u32x4 q4;
#pragma unroll
        for (int e = 0; e < 2; ++e) {
          auto sw = __builtin_amdgcn_permlane16_swap(o[2 * jp][e], o[2 * jp + 1][e], false, false);
          q4[e] = sw[0]; q4[2 + e] = sw[1];
        }
        const int f = fT * 64 + 16 * (2 * jp + (g & 1)) + 8 * (g >> 1);
        st16_dev(a.Gf + ((size_t)(dT * 56 + kpos(f >> 5)) * 4 + w) * 1024 + (((f & 31) >> 3) * 16 + c) * 16, q4);
      }
      grp_arrive(sy + 24, w, 1, lane);
      asm volatile("s_waitcnt vmcnt(0)" ::: "memory");
      grp_arrive(sy + 12, w, 2, lane);
      if (w == 0) {
        grp_wait<0>(sy + 12, 2);
        if (lane == 0) { st4_dev(a.fl + FL_G + (dT * 32 + fT) * GSP, tag); sy[9] = 1; }
      } else word_wait<0>(sy + 9, 1);
      w_issue<L_ * 2, 3>(wb2, vo, lring, XS2, WL);
      word_wait<0>(sy + 28 + w, 1);
      w_to_regs<RW2, 3, 0, XS2>(tra, wr2);
    } else {
      const int bq = r_ - 28;
      tH = __builtin_amdgcn_s_memrealtime();
#pragma unroll 1
      for (int li = tid; li < 1792; li += 256) {
        touch(lbase + X_DUM, (const char*)a.w2 + (size_t)((li >> 2) * 4 + bq) * (L_ * 2) + (xg * 4 + (li & 3)) * 128);
        asm volatile("s_waitcnt vmcnt(%0)" :: "i"(3) : "memory");
      }
      asm volatile("s_waitcnt vmcnt(0)" ::: "memory");
      grp_bar<2>(sy + 12, w, 2, lane);
      if (w == 0) {
        poll_flags4(a.fl + FL_H + lane * HQ, tag);
        if (lane == 0) sy[11] = 1;
      }
      grp_wait<2>(sy + 4, 3);
      w_offsets<L_ * 2>(lane, vo);
      w_issue<L_ * 2, 3>(wb2, vo, lring, 0, WL);
      asm volatile("s_waitcnt vmcnt(0)" ::: "memory");
      w_to_regs<RW2, 3, 0, XS2>(tra, wr2);
    }
    static_assert(XS2 <= WL - 3 && RW2 >= XS2 && RW2 <= WL, "");
    w_issue<L_ * 2, 3>(wb2, vo, lring, WL, XS2 + WL);
    if (RW2 > XS2) {
      asm volatile("s_waitcnt vmcnt(%0)" :: "i"(4 * (XS2 + WL - RW2)) : "memory");
      w_to_regs<RW2, 3, XS2, RW2>(tra, wr2);
      w_issue<L_ * 2, 3>(wb2, vo, lring, XS2 + WL, RW2 + WL);
    }
    if (w == 0) {
      if (!has1) {
#pragma unroll 1
        for (int sp = 0; sp < 4096 && __builtin_amdgcn_s_memrealtime() - tH < IDEL; ++sp) __builtin_amdgcn_s_sleep(4);
      }
      poll_flags<PBK>(a.fl + FL_G + (dT2 * 32 + (lane & 31)) * GSP, tag, lane >= 28);
      if (lane == 0) sy[11] = 2;
    } else word_wait<1>(sy + 11, 2);
    __builtin_amdgcn_s_setprio(3);
    gemm_main<14, L_ * 2, true, RW2, 3>(a.Gf + (size_t)(dT2 * 56 + w) * 4096, a.Gf + (size_t)(dT2 * 56 + (w ^ 2)) * 4096, wb2, vo, lring, tra, wr2, w, lane, acc,
                                        lds + w * (WL * 4096) + lane * 16, sy + 19);
    *(f32x4*)(lds + w * (WL * 4096) + 3 * 4096 + lane * 16) = acc[2][3];
    *(f32x4*)(lds + w * (WL * 4096) + 3 * 4096 + 1024 + lane * 16) = acc[3][3];
    grp_arrive(sy, w, 2, lane);
    EpiIn ei;
    epi_load(ei, a.x, a.b2, sTl, dT2, w, 0, lane);
    word_wait<0>(sy + 10, 1);
    epi_load_s(ei, lds + X_SC, w, 0, lane);
    grp_wait<0>(sy, 2);
    epi_finish(ei, acc[0][3], acc[1][3], lds, a.out, w, 0, lane);
  } else {
    const int j4 = w - 4;
    volatile LDSP unsigned* xb = sy + 4;
    asm volatile("s_waitcnt vmcnt(2)" : "+v"(praw[0]), "+v"(praw[1]), "+v"(praw[2]), "+v"(praw[3]), "+v"(praw[4]), "+v"(praw[5]), "+v"(praw[6]), "+v"(praw[7]) :: "memory");
    const int ch = j4 >> 1, hw = j4 & 1;
    const int d = tb * 2 + ch;
    {
      float* par = (float*)(lds + X_SC + j4 * 2048);
      float* tabf = (float*)(lds + X_SC + 8192 + ch * (TABN * 8));
      const float dt = rbf(__expf(bf_lo(praw[6])));
      const float zr = dt * bf_lo(praw[0]) * LOG2E, zi = dt * bf_lo(praw[1]) * 0.15915494309189535f;
      const float br = dt * bf_lo(praw[2]), bi = dt * bf_lo(praw[3]), cr = bf_lo(praw[4]), ci = bf_lo(praw[5]);
      float wr = cr * br - ci * bi, wi = cr * bi + ci * br;
      const float zmax = wave_max_nn(fmaxf(zr, 0.f));
      const float wmax = wave_max_nn(fmaxf(fabsf(wr), fabsf(wi)));
      const unsigned we = (__float_as_uint(wmax) >> 23) & 0xff;
      const unsigned wec = we < 16 ? 16 : (we > 250 ? 250 : we);
      const float wsc = __uint_as_float((266u - wec) << 23);
      const float osc = __uint_as_float((wec - 12u) << 23) * 6.103515625e-05f;
      const float zs = zr - zmax;
      wr *= wsc; wi *= wsc;
      float qr, qi, er, ei;
      cexp2(zs * 128.f, zi * 128.f, qr, qi);
      cexp2(zs * 1024.f, zi * 1024.f, er, ei);
      *(f32x4*)(par + lane * 8) = f32x4{zs, zi, wr, wi};
      *(f32x4*)(par + lane * 8 + 4) = f32x4{qr, qi, er, ei};
      const float p2r = er * er - ei * ei, p2i = 2.f * er * ei;
      const float k2048 = wave_sum(wr * p2r - wi * p2i) * 16384.f;
      const int r = lane & 15, g = lane >> 4;
      const float va = 8.f * r;
      const int off = r < 8 ? r : r < 12 ? 256 + 2 * (r - 8) : 512 + 256 * ((r - 12) >> 1) + 4 * (r & 1);
      const float vb = (float)off;
      f32x4 acc[2][2];
#pragma unroll
      for (int t = 0; t < 4; ++t) acc[t >> 1][t & 1] = f32x4{0.f, 0.f, 0.f, 0.f};
#pragma unroll 1
      for (int ks = 0; ks < 4; ++ks) {
        u32x4 a0, a1, b0, b1;
#pragma unroll
        for (int j = 0; j < 4; ++j) {
          const int n = ks * 16 + g * 4 + j;
          const f32x4 pp = *(const f32x4*)(par + n * 8), pq = *(const f32x4*)(par + n * 8 + 4);
          float tr, ti, xr_, xi_;
          cexp2(__builtin_fmaf(pp[0], va, 14.f), pp[1] * va, tr, ti);
          cexp2(pp[0] * vb, pp[1] * vb, xr_, xi_);
          const float ur = pp[2] * xr_ - pp[3] * xi_, ui = pp[2] * xi_ + pp[3] * xr_;
          a0[j] = pk_f16(tr, ti);
          a1[j] = pk_f16(tr * pq[0] - ti * pq[1], tr * pq[1] + ti * pq[0]);
          b0[j] = pk_f16(ur, -ui);
          b1[j] = pk_f16(ur * pq[2] - ui * pq[3], -(ur * pq[3] + ui * pq[2]));
        }
        acc[0][0] = __builtin_amdgcn_mfma_f32_16x16x32_f16(__builtin_bit_cast(f16x8, a0), __builtin_bit_cast(f16x8, b0), acc[0][0], 0, 0, 0);
        acc[1][0] = __builtin_amdgcn_mfma_f32_16x16x32_f16(__builtin_bit_cast(f16x8, a1), __builtin_bit_cast(f16x8, b0), acc[1][0], 0, 0, 0);
        acc[0][1] = __builtin_amdgcn_mfma_f32_16x16x32_f16(__builtin_bit_cast(f16x8, a0), __builtin_bit_cast(f16x8, b1), acc[0][1], 0, 0, 0);
        acc[1][1] = __builtin_amdgcn_mfma_f32_16x16x32_f16(__builtin_bit_cast(f16x8, a1), __builtin_bit_cast(f16x8, b1), acc[1][1], 0, 0, 0);
      }
      float kl = k2048 * osc;
#pragma unroll
      for (int t = 0; t < 4; ++t) acc[t >> 1][t & 1] *= osc;
      if (zmax > 0.f) {
#pragma unroll
        for (int t = 0; t < 4; ++t)
#pragma unroll
          for (int e = 0; e < 4; ++e)
            acc[t >> 1][t & 1][e] *= __builtin_amdgcn_exp2f(zmax * (vb + (float)(8 * (16 * (t >> 1) + e) + 1024 * (t & 1)) + 32.f * g));
        kl *= __builtin_amdgcn_exp2f(zmax * 2048.f);
      }
      {
        const int stride = r < 8 ? 8 : r < 12 ? 4 : 2;
        const int base = r < 8 ? r : r < 12 ? 256 + (r - 8) : 384 + 64 * ((r - 12) >> 1) + (r & 1);
        float* p0 = tabf + (base + stride * 4 * g) * 2 + hw;
#pragma unroll
        for (int it = 0; it < 2; ++it)
#pragma unroll
          for (int e = 0; e < 4; ++e) p0[stride * (16 * it + e) * 2] = acc[it][0][e];
        if ((off & 255) == 0) {
          float* p1 = tabf + (512 + (off >> 3) + 4 * g) * 2 + hw;
#pragma unroll
          for (int it = 0; it < 2; ++it)
#pragma unroll
            for (int e = 0; e < 4; ++e) p1[(16 * it + e) * 2] = acc[it][1][e];
        }
        if (lane < 2) tabf[(640 + lane) * 2 + hw] = lane ? 0.f : kl;
      }
      grp_arrive(xb, j4, 1, lane);
      const int l2 = hw * 64 + lane;
      char* lk = lds + X_KT + ch * KT_B;
      int ix[17];
#pragma unroll
      for (int e = 0; e < 17; e += 2) {
        const int j0 = 2047 - 16 * l2 - e, j1 = j0 - 1;
        const unsigned bb = pk_bf16((float)j0, (float)j1);
        const int i0 = (int)(bb & 0xffff) - (0x4300 - 128), i1 = (int)(bb >> 16) - (0x4300 - 128);
        ix[e] = j0 < 128 ? (j0 < 0 ? 641 : j0) : i0;
        if (e + 1 < 17) ix[e + 1] = j1 < 128 ? j1 : i1;
      }
      word_wait<1>(sy + 8, 1);
      if (l2 < 8) {
        const int pc = (l2 & 2) ? 258 + (l2 & 1) : (l2 & 1);
        *(u32x4*)(lk + (l2 >> 2) * R1OFF + pc * 16) = u32x4{0, 0, 0, 0};
      }
      if (j4 == 0) *(u32x4*)(lds + X_U + (lane & 1) * U_B + (lane >> 1) * 16) = u32x4{0, 0, 0, 0};
#pragma unroll
      for (int e = 0; e < 17; ++e) asm volatile("" : "+v"(ix[e]));
      grp_wait<0>(xb, 1);
      f32x2 tv[17];
#pragma unroll
      for (int e = 0; e < 17; ++e) tv[e] = *(const f32x2*)(tabf + ix[e] * 2);
      __builtin_amdgcn_sched_barrier(0);
      float val[17];
#pragma unroll
      for (int e = 0; e < 17; ++e) val[e] = tv[e][0] + tv[e][1];
#pragma unroll
      for (int hf = 0; hf < 2; ++hf) {
        u32x4 o0, o1;
#pragma unroll
        for (int e = 0; e < 4; ++e) { o0[e] = pk_bf16(val[8 * hf + 2 * e], val[8 * hf + 2 * e + 1]); o1[e] = pk_bf16(val[8 * hf + 2 * e + 1], val[8 * hf + 2 * e + 2]); }
        *(u32x4*)(lk + 32 + l2 * 32 + hf * 16) = o0;
        *(u32x4*)(lk + R1OFF + 32 + l2 * 32 + hf * 16) = o1;
      }
      if (l2 == 0) *(unsigned*)(lk + R1OFF + 28) = pk_bf16(0.f, val[0]);
    }
    {
      const float dsk = bf_lo(praw[7]);
      const char* lk = lds + X_KT + ch * KT_B;
      const char* lu = lds + X_U + ch * U_B;
      const int col = lane & 15, g = lane >> 4;
      const int p00 = 2047 - col + 8 * g;
      const char* gbase = lk + ((p00 & 1) ? R1OFF : 0) + (p00 >> 1) * 4 - 256 * hw;
      const char* bbase = lu + (UPAD - 16 + 16 * col + 8 * g) * 2 - 256 * hw;
      grp_bar<1>(xb, j4, 2, lane);
      bf16x8 gf[4][8], bf[2][8];
#pragma unroll
      for (int rr = 0; rr < 4; ++rr)
#pragma unroll
        for (int i = 0; i < 8; ++i) {
          const unsigned* gp = (const unsigned*)(gbase - 64 * (8 * i + rr));
          u32x4 gv = {gp[0], gp[1], gp[2], gp[3]};
          gf[rr][i] = __builtin_bit_cast(bf16x8, gv);
        }
#pragma unroll
      for (int rr = 0; rr < 4; ++rr)
        asm volatile("" : "+v"(gf[rr][0]), "+v"(gf[rr][1]), "+v"(gf[rr][2]), "+v"(gf[rr][3]), "+v"(gf[rr][4]), "+v"(gf[rr][5]), "+v"(gf[rr][6]), "+v"(gf[rr][7]));
      unsigned vo2[4];
      w_offsets<L_ * 2>(lane, vo2);
      word_wait(sy + 11, 1);
      if (has1) grp_wait<1>(sy + 20, 1);
      {
        u32x4 u0, u1;
        const int t0 = j4 * 64 + (lane >> 1);
        asm volatile("global_load_dwordx4 %0, %2, off sc1\n\tglobal_load_dwordx4 %1, %3, off sc1\n\ts_waitcnt vmcnt(0)" : "=&v"(u0), "=&v"(u1)
                     : "v"(a.X2 + ((size_t)t0 * 512 + tb * 2 + (lane & 1)) * 16), "v"(a.X2 + ((size_t)(t0 + 32) * 512 + tb * 2 + (lane & 1)) * 16) : "memory");
        char* lw = lds + X_U + (lane & 1) * U_B;
        *(u32x4*)(lw + UPAD * 2 + t0 * 16) = u0;
        *(u32x4*)(lw + UPAD * 2 + (t0 + 32) * 16) = u1;
      }
      grp_arrive(xb, j4, 3, lane);
      if (has1) w_issue<L_ * 2, 3>((const char*)a.w2 + (size_t)(j4 * 32) * (L_ * 2) + sTl * 128, vo2, lbase + j4 * (WL * 4096), 0, XS2);
      if (b == 0 && tid == 256) st4_dev(a.fl, tag);
      grp_wait<0>(xb, 3);
#define BLD(set, rr) do { _Pragma("unroll") for (int i = 0; i < 8; ++i) bf[set][i] = *(const bf16x8*)(bbase + 64 * (8 * i - (rr))); } while (0)
      BLD(0, 0);
      BLD(1, 1);
      f32x4 acc[8];
#pragma unroll
      for (int i = 0; i < 4; ++i) {
        const u32x2 uu = *(const u32x2*)(lu + (UPAD + 256 * (2 * i + hw) + 16 * col + 4 * g) * 2);
        const f32x4 uf = {bf_lo(uu[0]), bf_hi(uu[0]), bf_lo(uu[1]), bf_hi(uu[1])};
        if (hw) { acc[2 * i] = f32x4{0.f, 0.f, 0.f, 0.f}; acc[2 * i + 1] = uf * dsk; } else { acc[2 * i] = uf * dsk; acc[2 * i + 1] = f32x4{0.f, 0.f, 0.f, 0.f}; }
      }
      if (has1) {
        asm volatile("s_waitcnt vmcnt(0)" ::: "memory");
        if (lane == 0) sy[28 + j4] = 1;
        grp_wait<1>(sy + 24, 1);
      }
#pragma unroll
      for (int rr = 0; rr < 4; ++rr) {
#pragma unroll
        for (int bq = 0; bq < 8; ++bq)
#pragma unroll
          for (int i = 0; i + bq < 8; ++i) acc[i + bq] = __builtin_amdgcn_mfma_f32_16x16x32_bf16(gf[rr][bq], bf[rr & 1][i], acc[i + bq], 0, 0, 0);
        asm volatile("" : "+v"(acc[0]), "+v"(acc[1]), "+v"(acc[2]), "+v"(acc[3]), "+v"(acc[4]), "+v"(acc[5]), "+v"(acc[6]), "+v"(acc[7]) :: "memory");
        if (rr + 2 < 4) BLD(rr & 1, rr + 2);
        __builtin_amdgcn_sched_barrier(0);
      }
#undef BLD
      __builtin_amdgcn_s_setprio(3);
      char* xc = lds + X_SC + ch * 8192 + lane * 16;
      if (hw) {
#pragma unroll
        for (int i = 0; i < 4; ++i) { *(f32x4*)(xc + 4096 + i * 1024) = acc[2 * i]; acc[2 * i] = acc[2 * i + 1]; }
      } else {
#pragma unroll
        for (int i = 0; i < 4; ++i) *(f32x4*)(xc + i * 1024) = acc[2 * i + 1];
      }
      grp_arrive(xb, j4, 4, lane);
      word_wait<0>(xb + (j4 ^ 1), 4);
      u32x2 o[4];
#pragma unroll
      for (int i = 0; i < 4; ++i) {
        const f32x4 y = acc[2 * i] + *(const f32x4*)(xc + (hw ^ 1) * 4096 + i * 1024);
        o[i] = u32x2{pk_bf16(y[0], y[1]), pk_bf16(y[2], y[3])};
      }
#pragma unroll
      for (int ip = 0; ip < 2; ++ip) {
        u32x4 q4;
#pragma unroll
        for (int e = 0; e < 2; ++e) {
          auto sw = __builtin_amdgcn_permlane16_swap(o[2 * ip][e], o[2 * ip + 1][e], false, false);
          q4[e] = sw[0]; q4[2 + e] = sw[1];
        }
#pragma unroll
        for (int e = 0; e < 4; ++e) q4[e] = min(q4[e], 0xfffffffeu);
        const int t = 256 * (2 * (2 * ip + (g & 1)) + hw) + 16 * col + 8 * (g >> 1);
        st16_dev(a.sT + ((size_t)d * L_ + t) * 2, q4);
      }
      grp_arrive(xb, j4, 5, lane);
    }
    {
      __builtin_amdgcn_s_setprio(0);
      word_wait<2>(sy + 19, 1);
      grp_wait<1>(xb, 5);
      __builtin_amdgcn_s_setprio(3);
      u32x4 s0, s1;
      const int rw_ = j4 * 16 + (lane >> 3);
      const char* sp = a.sT + ((size_t)(dT2 * 64 + rw_) * L_ + sTl * 64) * 2 + (lane & 7) * 16;
#pragma unroll 1
      for (int att = 0; att < SPIN_CAP; ++att) {
        asm volatile("global_load_dwordx4 %0, %2, off sc1\n\tglobal_load_dwordx4 %1, %3, off sc1\n\ts_waitcnt vmcnt(0)" : "=&v"(s0), "=&v"(s1)
                     : "v"(sp), "v"(sp + (size_t)8 * L_ * 2) : "memory");
        const unsigned mx = max(max(max(s0[0], s0[1]), max(s0[2], s0[3])), max(max(s1[0], s1[1]), max(s1[2], s1[3])));
        if (!__any(mx == ~0u)) break;
        __builtin_amdgcn_s_sleep(8);
      }
      *(u32x4*)(lds + X_SC + rw_ * 144 + (lane & 7) * 16) = s0;
      *(u32x4*)(lds + X_SC + (rw_ + 8) * 144 + (lane & 7) * 16) = s1;
      grp_bar(xb, j4, 6, lane);
      if (tid == 256) sy[10] = 1;
      EpiIn ei;
      epi_load(ei, a.x, a.b2, sTl, dT2, j4, 2, lane);
      epi_load_s(ei, lds + X_SC, j4, 2, lane);
      asm volatile("s_waitcnt vmcnt(0) lgkmcnt(0)" ::: "memory");
      __builtin_amdgcn_s_setprio(0);
      grp_wait<1>(sy, 2);
      __builtin_amdgcn_s_setprio(3);
      const char* ow = lds + j4 * (WL * 4096) + 3 * 4096 + lane * 16;
      epi_finish(ei, *(const f32x4*)ow, *(const f32x4*)(ow + 1024), lds, a.out, j4, 2, lane);
    }
  }
}

__device__ __attribute__((noinline)) void code_end_marker() { asm volatile("s_nop 0"); }

extern "C" void kernel_launch(void* const* d_in, const int* in_sizes, int n_in, void* d_out, int out_size, void* d_ws,
                              size_t ws_size, hipStream_t stream) {
  char* ws = (char*)d_ws;
  Args a;
  a.x = (const u32x4*)d_in[0];
  a.Are = (cus)d_in[1]; a.Aim = (cus)d_in[2]; a.Bre = (cus)d_in[3]; a.Bim = (cus)d_in[4]; a.Cre = (cus)d_in[5]; a.Cim = (cus)d_in[6];
  a.logdt = (cus)d_in[7]; a.Dskip = (cus)d_in[8];
  a.w1 = (cus)d_in[10]; a.b1 = (cus)d_in[11]; a.w2 = (cus)d_in[12]; a.b2 = (cus)d_in[13];
  a.out = (unsigned short*)d_out;
  a.X2 = ws; a.Hf = ws + (2u << 20); a.Gf = ws + (4u << 20); a.sT = ws + (6u << 20); a.fl = (unsigned*)(ws + (8u << 20));
  (void)hipFuncSetAttribute((const void*)k_mega, hipFuncAttributeMaxDynamicSharedMemorySize, LDS_B);
  k_mega<<<256, 512, LDS_B, stream>>>(a);
}
```

```cpp
#include <hip/hip_runtime.h>
#include <hip/hip_bf16.h>
#include <stdint.h>

typedef short s16x4 __attribute__((ext_vector_type(4)));
typedef short bf16x8 __attribute__((ext_vector_type(8)));
typedef _Float16 f16x8 __attribute__((ext_vector_type(8)));
typedef _Float16 h2_t __attribute__((ext_vector_type(2)));
typedef __bf16 bf2_t __attribute__((ext_vector_type(2)));
typedef float f32x2 __attribute__((ext_vector_type(2)));
typedef float f32x4 __attribute__((ext_vector_type(4)));
typedef unsigned u32x2 __attribute__((ext_vector_type(2)));
typedef unsigned u32x4 __attribute__((ext_vector_type(4)));
typedef const unsigned short* cus;
#define DI __device__ __forceinline__
#define LDSP __attribute__((address_space(3)))

#define TW 4
#define RW1 5
#define RW2 5
#define WL 7
#define AD 3
#define XS2 4
#define PBK 6
#define HQ 16
#define GSP 32
#define GSTEP (16 - AD)
#define PSTEP 6
#define IDEL 800

constexpr int L_ = 2048, D_ = 512, N_ = 128, F_ = 1792;
constexpr int TABN = 648;
constexpr float LOG2E = 1.44269504088896341f;
constexpr int SPIN_CAP = 1 << 18;

DI unsigned pk_bf16(float lo, float hi) {
  f32x2 f = {lo, hi};
  return __builtin_bit_cast(unsigned, __builtin_convertvector(f, bf2_t));
}
DI unsigned pk_f16(float lo, float hi) {
  f32x2 f = {lo, hi};
  return __builtin_bit_cast(unsigned, __builtin_convertvector(f, h2_t));
}
DI float bf_lo(unsigned u) { return __uint_as_float(u << 16); }
DI float bf_hi(unsigned u) { return __uint_as_float(u & 0xffff0000u); }
DI float rbf(float x) { return bf_lo(pk_bf16(x, 0.f)); }

template <int CTRL, int RM, bool BC> DI float dppf(float v) {
  return __builtin_bit_cast(float, __builtin_amdgcn_update_dpp(0, __builtin_bit_cast(int, v), CTRL, RM, 0xf, BC));
}
DI float wave_sum(float v) {
  v += dppf<0x111, 0xf, true>(v);
  v += dppf<0x112, 0xf, true>(v);
  v += dppf<0x114, 0xf, true>(v);
  v += dppf<0x118, 0xf, true>(v);
  v += dppf<0x142, 0xa, false>(v);
  v += dppf<0x143, 0xc, false>(v);
  return __builtin_bit_cast(float, __builtin_amdgcn_readlane(__builtin_bit_cast(int, v), 63));
}
DI float wave_max_nn(float v) {
  v = fmaxf(v, dppf<0x111, 0xf, true>(v));
  v = fmaxf(v, dppf<0x112, 0xf, true>(v));
  v = fmaxf(v, dppf<0x114, 0xf, true>(v));
  v = fmaxf(v, dppf<0x118, 0xf, true>(v));
  v = fmaxf(v, dppf<0x142, 0xa, false>(v));
  v = fmaxf(v, dppf<0x143, 0xc, false>(v));
  return __builtin_bit_cast(float, __builtin_amdgcn_readlane(__builtin_bit_cast(int, v), 63));
}
DI void touch(unsigned ldum, const void* p) { asm volatile("s_mov_b32 m0, %0\n\ts_nop 0\n\tglobal_load_lds_dword %1, off" :: "s"(ldum), "v"(p) : "memory"); }
DI void lds_barrier() { asm volatile("s_waitcnt lgkmcnt(0)" ::: "memory"); __builtin_amdgcn_s_barrier(); asm volatile("" ::: "memory"); }
DI void st16_dev(void* p, u32x4 v) { asm volatile("global_store_dwordx4 %0, %1, off sc1\n\ts_nop 1" :: "v"(p), "v"(v) : "memory"); }
DI void st4_dev(void* p, unsigned v) { asm volatile("global_store_dword %0, %1, off sc1" :: "v"(p), "v"(v) : "memory"); }
DI unsigned ld4_dev_wait(const void* p) { unsigned r; asm volatile("global_load_dword %0, %1, off sc1\n\ts_waitcnt vmcnt(0)" : "=&v"(r) : "v"(p) : "memory"); return r; }

DI void grp_arrive(volatile LDSP unsigned* words, int idx, unsigned ph, int lane) {
  asm volatile("s_waitcnt lgkmcnt(0)" ::: "memory");
  if (lane == 0) words[idx] = ph;
}
template <int SL = 1>
DI void grp_wait(volatile LDSP unsigned* words, unsigned ph) {
#pragma unroll 1
  for (int sp = 0; sp < SPIN_CAP * 8; ++sp) {
    const u32x4 v = *(volatile LDSP u32x4*)words;
    const unsigned m = min(min(v[0], v[1]), min(v[2], v[3]));
    if (__builtin_amdgcn_readfirstlane(m) >= ph) break;
    if (SL) __builtin_amdgcn_s_sleep(SL);
  }
  asm volatile("" ::: "memory");
}
template <int SL = 1>
DI void grp_bar(volatile LDSP unsigned* words, int idx, unsigned ph, int lane) { grp_arrive(words, idx, ph, lane); grp_wait<SL>(words, ph); }
template <int SL = 1>
DI void word_wait(volatile LDSP unsigned* word, unsigned v) {
#pragma unroll 1
  for (int sp = 0; sp < SPIN_CAP * 8; ++sp) {
    if (__builtin_amdgcn_readfirstlane(*word) >= v) break;
    if (SL) __builtin_amdgcn_s_sleep(SL);
  }
  asm volatile("" ::: "memory");
}
template <int BK = PBK>
DI void poll_flags(const unsigned* p, unsigned tag, bool done) {
#pragma unroll 1
  for (int sp = 0; sp < SPIN_CAP; ++sp) {
    if (!done) done = ld4_dev_wait(p) == tag;
    if (__all(done)) break;
    __builtin_amdgcn_s_sleep(BK);
  }
}
DI void poll_flags4(const unsigned* p, unsigned tag) {
  bool done = false;
#pragma unroll 1
  for (int sp = 0; sp < SPIN_CAP; ++sp) {
    if (!done) {
      u32x4 f;
      asm volatile("global_load_dwordx4 %0, %1, off sc1\n\ts_waitcnt vmcnt(0)" : "=&v"(f) : "v"(p) : "memory");
      done = f[0] == tag && f[1] == tag && f[2] == tag && f[3] == tag;
    }
    if (__all(done)) break;
    __builtin_amdgcn_s_sleep(PBK);
  }
}

DI int kpos(int kb) { return kb ^ (((kb >> 2) & 1) << 1); }

template <bool PIVOT>
DI void ln8(const u32x4& in, u32x4& o) {
  f32x2 v[4];
#pragma unroll
  for (int i = 0; i < 4; ++i) v[i] = f32x2{bf_lo(in[i]), bf_hi(in[i])};
  const float p = PIVOT ? __builtin_bit_cast(float, __builtin_amdgcn_readfirstlane(__builtin_bit_cast(int, v[0][0]))) : 0.f;
  f32x2 t[4];
#pragma unroll
  for (int i = 0; i < 4; ++i) t[i] = PIVOT ? v[i] - p : v[i];
  const f32x2 s2 = (t[0] + t[1]) + (t[2] + t[3]);
  const f32x2 q2 = __builtin_elementwise_fma(t[0], t[0], t[1] * t[1]) + __builtin_elementwise_fma(t[2], t[2], t[3] * t[3]);
  const float ds = wave_sum(s2[0] + s2[1]) * (1.f / D_), dq = wave_sum(q2[0] + q2[1]) * (1.f / D_);
  const float mean = p + ds, var = fmaxf(__builtin_fmaf(-ds, ds, dq), 0.f);
  const float mb = rbf(mean), vb = rbf(var);
  const float den = rbf(__builtin_amdgcn_sqrtf(rbf(vb + 1e-5f)));
  const float inv = __builtin_amdgcn_rcpf(den);
#pragma unroll
  for (int i = 0; i < 4; ++i) {
    const f32x2 c = v[i] - mb;
    const unsigned r = pk_bf16(c[0], c[1]);
    const f32x2 n = f32x2{bf_lo(r), bf_hi(r)} * inv;
    o[i] = pk_bf16(n[0], n[1]);
  }
}
DI void cexp2(float le, float ph, float& re, float& im) {
  const float e = __builtin_amdgcn_exp2f(le);
  const float p = __builtin_amdgcn_fractf(ph);
  re = e * __builtin_amdgcn_cosf(p);
  im = e * __builtin_amdgcn_sinf(p);
}
DI f32x2 xsig2(f32x2 x, f32x2 e) {
  const f32x2 d = f32x2{__builtin_amdgcn_exp2f(e[0]), __builtin_amdgcn_exp2f(e[1])} + 1.f;
  return x * f32x2{__builtin_amdgcn_rcpf(d[0]), __builtin_amdgcn_rcpf(d[1])};
}
DI f32x2 gelu_tanh2(f32x2 x) {
  constexpr float k1 = -2.f * 0.7978845608028654f * LOG2E, k2 = k1 * 0.044715f;
  return xsig2(x, x * __builtin_elementwise_fma(x * x, f32x2{k2, k2}, f32x2{k1, k1}));
}
DI f32x2 silu2(f32x2 x) { return xsig2(x, x * -LOG2E); }
DI f32x2 bf_pair(unsigned u) { return f32x2{bf_lo(u), bf_hi(u)}; }

#define DMA4(ldsaddr, vo, sb) do { unsigned keep_; \
    asm volatile("s_mov_b32 %0, m0\n\ts_mov_b32 m0, %1\n\ts_nop 0\n\t" \
                 "global_load_lds_dwordx4 %2, %6\n\tglobal_load_lds_dwordx4 %3, %6 offset:1024\n\t" \
                 "global_load_lds_dwordx4 %4, %6 offset:2048\n\tglobal_load_lds_dwordx4 %5, %6 offset:3072\n\ts_mov_b32 m0, %0" \
                 : "=&s"(keep_) : "s"(__builtin_amdgcn_readfirstlane(ldsaddr)), "v"(vo[0]), "v"(vo[1]), "v"(vo[2]), "v"(vo[3]), "s"(sb) : "memory"); } while (0)
template <int RB> DI void w_offsets(int lane, unsigned (&vo)[4]) {
  const int lr = lane >> 3, p8 = lane & 7;
#pragma unroll
  for (int j = 0; j < 4; ++j) vo[j] = (8 * j + lr) * RB + ((p8 ^ ((lr & 2) | ((j & 1) << 2))) << 4) - 1024 * j;
}
struct TrAddr { const char* p[4]; };
DI void tr_addr(TrAddr& t, const char* ring, int lane) {
  const int g = lane >> 4, q = (lane & 15) >> 2, p = lane & 3;
  const int frow = (q & 2) | ((g & 1) << 2);
  const char* trb = ring + (8 * g + q) * 128 + (p & 1) * 8;
#pragma unroll
  for (int j = 0; j < 4; ++j) t.p[j] = trb + (((2 * j + (p >> 1)) ^ frow) << 4);
}
DI void tr_read(bf16x8 (&fr)[4], const TrAddr& t, int off) {
#pragma unroll
  for (int j = 0; j < 4; ++j) {
    s16x4 lo = __builtin_amdgcn_ds_read_tr16_b64_v4i16((LDSP s16x4*)(t.p[j] + off));
    s16x4 hi = __builtin_amdgcn_ds_read_tr16_b64_v4i16((LDSP s16x4*)(t.p[j] + off + 512));
    fr[j] = __builtin_shufflevector(lo, hi, 0, 1, 2, 3, 4, 5, 6, 7);
  }
}
template <int RB, int OFF>
DI void w_issue(const char* wb, const unsigned (&vo)[4], unsigned lring, int k0, int k1) {
#pragma unroll 1
  for (int k = k0; k < k1; ++k) DMA4(lring + ((k + OFF) % WL) * 4096, vo, wb + (size_t)k * 128 * RB);
}
template <int RWN, int OFF, int K0 = 0, int K1 = RWN>
DI void w_to_regs(const TrAddr& tra, bf16x8 (&wr)[RWN][4]) {
#pragma unroll
  for (int k = K0; k < K1; ++k) tr_read(wr[k], tra, ((k + OFF) % WL) * 4096);
#pragma unroll
  for (int k = K0; k < K1; ++k) asm volatile("s_waitcnt lgkmcnt(0)" : "+v"(wr[k][0]), "+v"(wr[k][1]), "+v"(wr[k][2]), "+v"(wr[k][3]) :: "memory");
}
template <int NKB, int RWN, int OFF> struct Sch {
  static constexpr int NL = NKB - RWN;
  static constexpr int blk(int i) { return i < NL ? RWN + i : i - NL; }
  static constexpr int slot(int i) { return (RWN + i + OFF) % WL; }
  static constexpr bool dma(int s) { return s >= 1 && s - 1 + WL < NL; }
  static constexpr int younger(int i) {
    int pos = 0, pa = -1, pw = -1;
    for (int k = 0; k < AD; ++k) { if (k == i) pa = pos; ++pos; }
    for (int s = 0; s < i; ++s) {
      if (dma(s)) { if (s - 1 + WL == i + 1) pw = pos; ++pos; }
      if (s + AD < NKB) { if (s + AD == i) pa = pos; ++pos; }
    }
    const int need = pa > pw ? pa : pw;
    return pos - 1 - need;
  }
};
DI bf16x8 tr_frag(const char* p) {
  s16x4 lo = __builtin_amdgcn_ds_read_tr16_b64_v4i16((LDSP s16x4*)(p));
  s16x4 hi = __builtin_amdgcn_ds_read_tr16_b64_v4i16((LDSP s16x4*)(p + 512));
  return __builtin_shufflevector(lo, hi, 0, 1, 2, 3, 4, 5, 6, 7);
}
template <int NKB, int RB, bool SWAP, int RWN, int OFF>
DI void gemm_main(const char* ab, const char* abo, const char* wb, const unsigned (&vo)[4], unsigned lring, const TrAddr& tra,
                  bf16x8 (&wr)[RWN][4], int w, int lane, f32x4 (&acc)[4][4], char* park, volatile LDSP unsigned* sig) {
  typedef Sch<NKB, RWN, OFF> S;
  unsigned va[4];
#pragma unroll
  for (int t = 0; t < 4; ++t) va[t] = lane * 16 + ((t + w + 1) & 3) * 1024;
  u32x4 av[AD][4];
  bf16x8 fr[4];
#define ABASE(i) (((S::blk(i) & 1) ? abo : ab) + (size_t)S::blk(i) * 16384)
#define LDA1(i, t) asm volatile("global_load_dwordx4 %0, %1, %2 sc1" : "=&v"(av[(i) % AD][t]) : "v"(va[t]), "s"(ABASE(i)) : "memory")
#define DMA1(i, q) asm volatile("s_mov_b32 m0, %0\n\ts_nop 0\n\tglobal_load_lds_dwordx4 %1, %2 offset:%3" \
                 :: "s"(__builtin_amdgcn_readfirstlane(lring + S::slot((i) - 1) * 4096)), "v"(vo[q]), "s"(wb + (size_t)(RWN + (i) - 1 + WL) * 128 * RB), "i"((q) * 1024) : "memory")
#pragma unroll
  for (int i = 0; i < AD; ++i) { LDA1(i, 0); LDA1(i, 1); LDA1(i, 2); LDA1(i, 3); }
  asm volatile("s_waitcnt vmcnt(%0)" :: "i"(4 * AD) : "memory");
#pragma unroll
  for (int j = 0; j < 4; ++j) fr[j] = tr_frag(tra.p[j] + S::slot(0) * 4096);
#pragma unroll
  for (int i = 0; i < NKB; ++i) {
    const int st = i % AD;
    asm volatile("s_waitcnt vmcnt(%4)" : "+v"(av[st][0]), "+v"(av[st][1]), "+v"(av[st][2]), "+v"(av[st][3]) : "i"(4 * S::younger(i)) : "memory");
    if (i == (SWAP ? PSTEP : GSTEP) && lane == 0) *sig = 1;
    if (i == NKB - 1) {
#pragma unroll
      for (int m = 0; m < 16; ++m) {
        const int t = m >> 2, j = m & 3;
        if (SWAP) asm volatile("v_mfma_f32_16x16x32_bf16 %0, %1, %2, %0" : "+v"(acc[j][t]) : "v"(av[st][t]), "v"(wr[RWN - 1][j]) : "memory");
        else asm volatile("v_mfma_f32_16x16x32_bf16 %0, %1, %2, %0" : "+v"(acc[j][t]) : "v"(wr[RWN - 1][j]), "v"(av[st][t]) : "memory");
        if (m >= 8) *(f32x4*)(park + (m - 8) * 1024) = acc[(m - 8) & 3][(m - 8) >> 2];
      }
      continue;
    }
#pragma unroll
    for (int j = 0; j < 4; ++j) {
#pragma unroll
      for (int t = 0; t < 4; ++t) {
        const bf16x8 wf = i < S::NL ? fr[j] : wr[i < S::NL ? 0 : i - S::NL][j];
        if (i == 0) {
          if (SWAP) asm volatile("v_mfma_f32_16x16x32_bf16 %0, %1, %2, 0" : "=&v"(acc[j][t]) : "v"(av[st][t]), "v"(wf) : "memory");
          else asm volatile("v_mfma_f32_16x16x32_bf16 %0, %1, %2, 0" : "=&v"(acc[j][t]) : "v"(wf), "v"(av[st][t]) : "memory");
        } else {
          if (SWAP) asm volatile("v_mfma_f32_16x16x32_bf16 %0, %1, %2, %0" : "+v"(acc[j][t]) : "v"(av[st][t]), "v"(wf) : "memory");
          else asm volatile("v_mfma_f32_16x16x32_bf16 %0, %1, %2, %0" : "+v"(acc[j][t]) : "v"(wf), "v"(av[st][t]) : "memory");
        }
        if (S::dma(i) && j < 2 && (t == 0 || t == 2)) { if (j == 0 && t == 0) DMA1(i, 0); else if (j == 0) DMA1(i, 1); else if (t == 0) DMA1(i, 2); else DMA1(i, 3); }
        if (j == 3 && i + AD < NKB) { if (t == 0) LDA1(i + AD, 0); else if (t == 1) LDA1(i + AD, 1); else if (t == 2) LDA1(i + AD, 2); else LDA1(i + AD, 3); }
      }
      if (i + 1 < S::NL) fr[j] = tr_frag(tra.p[j] + S::slot(i + 1) * 4096);
    }
  }
#pragma unroll
  for (int j = 0; j < 4; ++j) { *(f32x4*)(park + (8 + j) * 1024) = acc[j][2]; asm volatile("" ::: "memory"); }
  asm volatile("s_nop 7\n\ts_nop 7" ::: "memory");
#undef LDA1
#undef DMA1
#undef ABASE
}
DI void reduce4(f32x4 (&acc)[4][4], f32x4 (&res)[4], char* lds, int w, int lane, volatile LDSP unsigned* words, unsigned ph) {
  grp_bar<0>(words, w, ph, lane);
  f32x4 pv[3][4];
#pragma unroll
  for (int k = 1; k < 4; ++k) {
    const char* p = lds + ((w + k) & 3) * (WL * 4096) + (3 - k) * 4096 + lane * 16;
#pragma unroll
    for (int j = 0; j < 4; ++j) pv[k - 1][j] = *(const f32x4*)(p + j * 1024);
  }
#pragma unroll
  for (int j = 0; j < 4; ++j) res[j] = (acc[j][3] + pv[0][j]) + (pv[1][j] + pv[2][j]);
}

struct EpiIn { u32x2 xv[2], sv[2]; unsigned braw[2]; size_t oo; };
DI void epi_load(EpiIn& e, const u32x4* x, cus b2, int sTl, int dT2, int w, int j0, int lane) {
  const int g = lane >> 4, c = lane & 15;
  e.oo = (size_t)(sTl * 64 + 16 * j0 + c) * D_ + dT2 * 64 + 16 * w + 4 * g;
#pragma unroll
  for (int j = 0; j < 2; ++j) {
    e.braw[j] = b2[sTl * 64 + 16 * (j0 + j) + c];
    e.xv[j] = *(const u32x2*)((cus)x + e.oo + (size_t)(16 * j) * D_);
  }
}
DI void epi_load_s(EpiIn& e, const char* st, int w, int j0, int lane) {
#pragma unroll
  for (int j = 0; j < 2; ++j)
    e.sv[j] = __builtin_bit_cast(u32x2, __builtin_amdgcn_ds_read_tr16_b64_v4i16((LDSP s16x4*)(st + (16 * w + (lane >> 2)) * 144 + (j0 + j) * 32 + (lane & 3) * 8)));
}
DI void epi_finish(const EpiIn& e, f32x4 own0, f32x4 own1, const char* lds, unsigned short* out, int w, int j0, int lane) {
  f32x4 pv[3][2];
#pragma unroll
  for (int k = 1; k < 4; ++k) {
    const char* p = lds + ((w + k) & 3) * (WL * 4096) + (3 - k) * 4096 + j0 * 1024 + lane * 16;
#pragma unroll
    for (int j = 0; j < 2; ++j) pv[k - 1][j] = *(const f32x4*)(p + j * 1024);
  }
#pragma unroll
  for (int j = 0; j < 2; ++j) {
    const f32x4 r = ((j ? own1 : own0) + pv[0][j]) + (pv[1][j] + pv[2][j]) + bf_lo(e.braw[j]);
    const f32x2 o0 = __builtin_elementwise_fma(bf_pair(e.sv[j][0]), silu2(f32x2{r[0], r[1]}), bf_pair(e.xv[j][0]));
    const f32x2 o1 = __builtin_elementwise_fma(bf_pair(e.sv[j][1]), silu2(f32x2{r[2], r[3]}), bf_pair(e.xv[j][1]));
    *(u32x2*)(out + e.oo + (size_t)(16 * j) * D_) = u32x2{pk_bf16(o0[0], o0[1]), pk_bf16(o1[0], o1[1])};
  }
}

struct Args {
  const u32x4* x; cus Are, Aim, Bre, Bim, Cre, Cim, logdt, Dskip, w1, b1, w2, b2;
  unsigned short* out;
  char* Hf;  char* X2; char* Gf; char* sT; unsigned* fl;
};
constexpr int FL_H = 64, FL_G = 2048;

constexpr int KR = 2080;
constexpr int UPAD = 256;
constexpr int R1OFF = KR * 2 + 128;
constexpr int KT_B = R1OFF + KR * 2 + 32;
constexpr int U_B = (UPAD + L_) * 2;
constexpr int XR = 4 * WL * 4096;
constexpr int X_KT = XR;
constexpr int X_U = X_KT + 2 * KT_B;
constexpr int X_SC = X_U + 2 * U_B;
constexpr int SC_B = 8192 + 2 * TABN * 8;
constexpr int X_SY = X_SC + SC_B;
constexpr int X_B1 = X_SY + 128;
constexpr int X_DUM = X_B1 + 512;
constexpr int LDS_B = X_DUM + 256;
static_assert(LDS_B <= 160 * 1024 && SC_B >= 16384 && 2 * KT_B >= 16 * (1024 + 32), "LDS");

__device__ __attribute__((noinline)) void code_end_marker();

__global__ __launch_bounds__(512, 2) void k_mega(Args a) {
  extern __shared__ __attribute__((aligned(16))) char lds[];
  const int tid = threadIdx.x, lane = tid & 63, w = __builtin_amdgcn_readfirstlane(tid >> 6);
  const int b = blockIdx.x, xg = b & 7, r_ = b >> 3;
  const int tb = xg * 32 + r_;
  const unsigned lbase = (unsigned)(size_t)(LDSP char*)lds;
  volatile LDSP unsigned* sy = (volatile LDSP unsigned*)(lds + X_SY);
  const bool has1 = r_ < 28;
  const int idx1 = xg * 28 + (has1 ? r_ : 0);
  const int fT = idx1 >> 3, dT = idx1 & 7;
  const int sTl = tb >> 3, dT2 = tb & 7;
  u32x4 xr; unsigned ep;
  asm volatile("global_load_dwordx4 %0, %2, %3\n\tglobal_load_dword %1, %4, %5 sc1" : "=&v"(xr), "=&v"(ep)
               : "v"(lane * 16), "s"(a.x + (size_t)(tb * 8 + w) * 64), "v"(0), "s"(a.fl) : "memory");
  if (tid < 32) sy[tid] = 0;
  unsigned praw[8];
  if (w >= 4) {
    const int i = (tb * 2) * N_ + (w - 4) * 64;
    const int dch = tb * 2 + ((w - 4) >> 1);
    asm volatile("global_load_ushort %0, %8, %10\n\tglobal_load_ushort %1, %8, %11\n\tglobal_load_ushort %2, %8, %12\n\tglobal_load_ushort %3, %8, %13\n\t"
                 "global_load_ushort %4, %8, %14\n\tglobal_load_ushort %5, %8, %15\n\tglobal_load_ushort %6, %9, %16\n\tglobal_load_ushort %7, %9, %17\n\ts_barrier"
                 : "=&v"(praw[0]), "=&v"(praw[1]), "=&v"(praw[2]), "=&v"(praw[3]), "=&v"(praw[4]), "=&v"(praw[5]), "=&v"(praw[6]), "=&v"(praw[7])
                 : "v"(lane * 2), "v"(0), "s"(a.Are + i), "s"(a.Aim + i), "s"(a.Bre + i), "s"(a.Bim + i), "s"(a.Cre + i), "s"(a.Cim + i),
                   "s"(a.logdt + dch), "s"(a.Dskip + dch) : "memory");
    const int li = r_ * 256 + tid - 256, ft0 = (xg * 28) >> 3;
    const char* t1 = (const char*)a.w1 + (size_t)(li >> 2) * (F_ * 2) + (ft0 + (li & 3)) * 128;
    const char* t2 = t1;
    {
      unsigned long long pc;
      asm volatile("s_getpc_b64 %0" : "=s"(pc));
      const unsigned long long pe = (unsigned long long)(void*)&code_end_marker;
      const unsigned long long pa = (pc & ~127ull) + (unsigned long long)(((w - 4) * 4 + (r_ & 3)) * 64 + lane) * 128;
      if (pe > pc && pe - pc < 65536 && pa + 4 <= pe && r_ >= 28 && w < 6) t2 = (const char*)pa;
    }
    const char* t3 = lane < 32 ? a.X2 + (size_t)lane * 262144 : lane < 40 ? (const char*)a.out + (size_t)(lane - 32) * 262144
                   : lane < 41 ? (const char*)a.b2 : (const char*)a.w2 + (size_t)(lane - 41) * 319488;
    if (w != TW) t3 = t1;
    asm volatile("s_mov_b32 m0, %2\n\ts_nop 0\n\tglobal_load_lds_dword %3, off\n\tglobal_load_lds_dword %1, off\n\tglobal_load_lds_dword %0, off\n\ts_waitcnt vmcnt(11)"
                 :: "v"(t1), "v"(t2), "s"(lbase + X_DUM), "v"(t3) : "memory");
  } else {
    u32x4 bv;
    const u32x4 mark = {~0u, ~0u, ~0u, ~0u};
    asm volatile("global_load_dwordx4 %0, %1, %2\n\ts_barrier\n\tglobal_store_dwordx4 %3, %4, %5 sc1\n\tglobal_store_dwordx4 %3, %4, %5 offset:1024 sc1\n\t"
                 "s_waitcnt vmcnt(2)"
                 : "=&v"(bv) : "v"((lane & 7) * 16), "s"(a.b1 + fT * 64), "v"(lane * 16), "v"(mark), "s"(a.sT + (size_t)tb * 8192 + w * 2048) : "memory");
    if (lane < 8) *(u32x4*)(lds + X_B1 + w * 128 + lane * 16) = bv;
  }
  asm volatile("" : "+v"(xr), "+v"(ep));
  const unsigned tag = __builtin_amdgcn_readfirstlane(ep) + 1u;
  constexpr int RS = 1024 + 32;
  {
    u32x4 o1, o2;
    ln8<true>(xr, o1);
    *(u32x4*)(lds + X_KT + w * RS + lane * 16) = o1;
    ln8<false>(o1, o2);
    *(u32x4*)(lds + X_KT + (8 + w) * RS + lane * 16) = o2;
  }
  lds_barrier();

  if (__builtin_expect(w < 4, 1)) {
    __builtin_amdgcn_s_setprio(3);
    const int g = lane >> 4, c = lane & 15, q = (lane & 15) >> 2, p = lane & 3;
    {
      u32x4 ov[2][2];
#pragma unroll
      for (int arr = 1; arr >= 0; --arr)
#pragma unroll
        for (int it = 0; it < 2; ++it) {
          const char* base = lds + X_KT + (arr * 8 + q) * RS + (w * 8 + it * 4 + g) * 32 + p * 8;
          s16x4 lo = __builtin_amdgcn_ds_read_tr16_b64_v4i16((LDSP s16x4*)(base));
          s16x4 hi = __builtin_amdgcn_ds_read_tr16_b64_v4i16((LDSP s16x4*)(base + 4 * RS));
          ov[arr][it] = __builtin_bit_cast(u32x4, __builtin_shufflevector(lo, hi, 0, 1, 2, 3, 4, 5, 6, 7));
        }
#pragma unroll
      for (int arr = 1; arr >= 0; --arr)
#pragma unroll
        for (int it = 0; it < 2; ++it) {
          const int dt = w * 8 + it * 4 + g;
          if (arr) st16_dev(a.Hf + ((size_t)((dt >> 2) * 64 + kpos(tb >> 2)) * 4 + (dt & 3)) * 1024 + ((tb & 3) * 16 + c) * 16, ov[arr][it]);
          else st16_dev(a.X2 + ((size_t)tb * 512 + dt * 16 + c) * 16, ov[arr][it]);
        }
    }
    const unsigned lring = lbase + w * (WL * 4096);
    TrAddr tra;
    tr_addr(tra, lds + w * (WL * 4096), lane);
    f32x4 acc[4][4], res[4];
    unsigned vo[4];
    w_offsets<F_ * 2>(lane, vo);
    const char* wb1 = (const char*)a.w1 + (size_t)(w * 32) * (F_ * 2) + fT * 128;
    const char* wb2 = (const char*)a.w2 + (size_t)(w * 32) * (L_ * 2) + sTl * 128;
    unsigned long long tH = 0;
    bf16x8 wr2[RW2][4];
    asm volatile("s_waitcnt vmcnt(0)" ::: "memory");
    grp_arrive(sy + 12, w, 1, lane);
    if (w == 0) {
      grp_wait<0>(sy + 12, 1);
      if (lane == 0) { st4_dev(a.fl + FL_H + (tb >> 2) * HQ + (tb & 3), tag); sy[8] = 1; }
    } else word_wait<0>(sy + 8, 1);
    if (!has1) __builtin_amdgcn_s_setprio(0);
    if (has1) {
      static_assert(GSTEP >= 16 - RW1, "the ring must be drained before it is handed over");
      bf16x8 wr1[RW1][4];
      w_issue<F_ * 2, 0>(wb1, vo, lring, 0, WL);
      asm volatile("s_waitcnt vmcnt(%0)" :: "i"(4 * (WL - RW1)) : "memory");
      w_to_regs<RW1, 0>(tra, wr1);
      w_issue<F_ * 2, 0>(wb1, vo, lring, WL, WL + RW1);
      if (w == 0) {
        poll_flags4(a.fl + FL_H + lane * HQ, tag);
        if (lane == 0) sy[11] = 1;
      } else word_wait<1>(sy + 11, 1);
      gemm_main<16, F_ * 2, false, RW1, 0>(a.Hf + (size_t)(dT * 64 + w) * 4096, a.Hf + (size_t)(dT * 64 + (w ^ 2)) * 4096, wb1, vo, lring, tra, wr1, w, lane, acc,
                                           lds + w * (WL * 4096) + lane * 16, sy + 20 + w);
      w_offsets<L_ * 2>(lane, vo);
      reduce4(acc, res, lds, w, lane, sy, 1);
      u32x2 o[4];
#pragma unroll
      for (int j = 0; j < 4; ++j) {
        const u32x2 bb = *(const u32x2*)(lds + X_B1 + w * 128 + (16 * j + 4 * g) * 2);
        const f32x2 v0 = gelu_tanh2(f32x2{res[j][0], res[j][1]} + bf_pair(bb[0])), v1 = gelu_tanh2(f32x2{res[j][2], res[j][3]} + bf_pair(bb[1]));
        o[j] = u32x2{pk_bf16(v0[0], v0[1]), pk_bf16(v1[0], v1[1])};
      }
#pragma unroll
      for (int jp = 0; jp < 2; ++jp) {
        u32x4 q4;
#pragma unroll
        for (int e = 0; e < 2; ++e) {
          auto sw = __builtin_amdgcn_permlane16_swap(o[2 * jp][e], o[2 * jp + 1][e], false, false);
          q4[e] = sw[0]; q4[2 + e] = sw[1];
        }
        const int f = fT * 64 + 16 * (2 * jp + (g & 1)) + 8 * (g >> 1);
        st16_dev(a.Gf + ((size_t)(dT * 56 + kpos(f >> 5)) * 4 + w) * 1024 + (((f & 31) >> 3) * 16 + c) * 16, q4);
      }
      grp_arrive(sy + 24, w, 1, lane);
      asm volatile("s_waitcnt vmcnt(0)" ::: "memory");
      grp_arrive(sy + 12, w, 2, lane);
      if (w == 0) {
        grp_wait<0>(sy + 12, 2);
        if (lane == 0) { st4_dev(a.fl + FL_G + (dT * 32 + fT) * GSP, tag); sy[9] = 1; }
      } else word_wait<0>(sy + 9, 1);
      w_issue<L_ * 2, 3>(wb2, vo, lring, XS2, WL);
      word_wait<0>(sy + 28 + w, 1);
      w_to_regs<RW2, 3, 0, XS2>(tra, wr2);
    } else {
      const int bq = r_ - 28;
      tH = __builtin_amdgcn_s_memrealtime();
#pragma unroll 1
      for (int li = tid; li < 1792; li += 256) {
        touch(lbase + X_DUM, (const char*)a.w2 + (size_t)((li >> 2) * 4 + bq) * (L_ * 2) + (xg * 4 + (li & 3)) * 128);
        asm volatile("s_waitcnt vmcnt(%0)" :: "i"(3) : "memory");
      }
      asm volatile("s_waitcnt vmcnt(0)" ::: "memory");
      grp_bar<2>(sy + 12, w, 2, lane);
      if (w == 0) {
        poll_flags4(a.fl + FL_H + lane * HQ, tag);
        if (lane == 0) sy[11] = 1;
      }
      grp_wait<2>(sy + 4, 3);
      w_offsets<L_ * 2>(lane, vo);
      w_issue<L_ * 2, 3>(wb2, vo, lring, 0, WL);
      asm volatile("s_waitcnt vmcnt(0)" ::: "memory");
      w_to_regs<RW2, 3, 0, XS2>(tra, wr2);
    }
    static_assert(XS2 <= WL - 3 && RW2 >= XS2 && RW2 <= WL, "");
    w_issue<L_ * 2, 3>(wb2, vo, lring, WL, XS2 + WL);
    if (RW2 > XS2) {
      asm volatile("s_waitcnt vmcnt(%0)" :: "i"(4 * (XS2 + WL - RW2)) : "memory");
      w_to_regs<RW2, 3, XS2, RW2>(tra, wr2);
      w_issue<L_ * 2, 3>(wb2, vo, lring, XS2 + WL, RW2 + WL);
    }
    if (w == 0) {
      if (!has1) {
#pragma unroll 1
        for (int sp = 0; sp < 4096 && __builtin_amdgcn_s_memrealtime() - tH < IDEL; ++sp) __builtin_amdgcn_s_sleep(4);
      }
      poll_flags<PBK>(a.fl + FL_G + (dT2 * 32 + (lane & 31)) * GSP, tag, lane >= 28);
      if (lane == 0) sy[11] = 2;
    } else word_wait<1>(sy + 11, 2);
    __builtin_amdgcn_s_setprio(3);
    gemm_main<14, L_ * 2, true, RW2, 3>(a.Gf + (size_t)(dT2 * 56 + w) * 4096, a.Gf + (size_t)(dT2 * 56 + (w ^ 2)) * 4096, wb2, vo, lring, tra, wr2, w, lane, acc,
                                        lds + w * (WL * 4096) + lane * 16, sy + 19);
    *(f32x4*)(lds + w * (WL * 4096) + 3 * 4096 + lane * 16) = acc[2][3];
    *(f32x4*)(lds + w * (WL * 4096) + 3 * 4096 + 1024 + lane * 16) = acc[3][3];
    grp_arrive(sy, w, 2, lane);
    EpiIn ei;
    epi_load(ei, a.x, a.b2, sTl, dT2, w, 0, lane);
    word_wait<0>(sy + 10, 1);
    epi_load_s(ei, lds + X_SC, w, 0, lane);
    grp_wait<0>(sy, 2);
    epi_finish(ei, acc[0][3], acc[1][3], lds, a.out, w, 0, lane);
  } else {
    const int j4 = w - 4;
    volatile LDSP unsigned* xb = sy + 4;
    asm volatile("s_waitcnt vmcnt(3)" : "+v"(praw[0]), "+v"(praw[1]), "+v"(praw[2]), "+v"(praw[3]), "+v"(praw[4]), "+v"(praw[5]), "+v"(praw[6]), "+v"(praw[7]) :: "memory");
    const int ch = j4 >> 1, hw = j4 & 1;
    const int d = tb * 2 + ch;
    {
      float* par = (float*)(lds + X_SC + j4 * 2048);
      float* tabf = (float*)(lds + X_SC + 8192 + ch * (TABN * 8));
      const float dt = rbf(__expf(bf_lo(praw[6])));
      const float zr = dt * bf_lo(praw[0]) * LOG2E, zi = dt * bf_lo(praw[1]) * 0.15915494309189535f;
      const float br = dt * bf_lo(praw[2]), bi = dt * bf_lo(praw[3]), cr = bf_lo(praw[4]), ci = bf_lo(praw[5]);
      float wr = cr * br - ci * bi, wi = cr * bi + ci * br;
      const float zmax = wave_max_nn(fmaxf(zr, 0.f));
      const float wmax = wave_max_nn(fmaxf(fabsf(wr), fabsf(wi)));
      const unsigned we = (__float_as_uint(wmax) >> 23) & 0xff;
      const unsigned wec = we < 16 ? 16 : (we > 250 ? 250 : we);
      const float wsc = __uint_as_float((266u - wec) << 23);
      const float osc = __uint_as_float((wec - 12u) << 23) * 6.103515625e-05f;
      const float zs = zr - zmax;
      wr *= wsc; wi *= wsc;
      float qr, qi, er, ei;
      cexp2(zs * 128.f, zi * 128.f, qr, qi);
      cexp2(zs * 1024.f, zi * 1024.f, er, ei);
      *(f32x4*)(par + lane * 8) = f32x4{zs, zi, wr, wi};
      *(f32x4*)(par + lane * 8 + 4) = f32x4{qr, qi, er, ei};
      const float p2r = er * er - ei * ei, p2i = 2.f * er * ei;
      const float k2048 = wave_sum(wr * p2r - wi * p2i) * 16384.f;
      const int r = lane & 15, g = lane >> 4;
      const float va = 8.f * r;
      const int off = r < 8 ? r : r < 12 ? 256 + 2 * (r - 8) : 512 + 256 * ((r - 12) >> 1) + 4 * (r & 1);
      const float vb = (float)off;
      f32x4 acc[2][2];
#pragma unroll
      for (int t = 0; t < 4; ++t) acc[t >> 1][t & 1] = f32x4{0.f, 0.f, 0.f, 0.f};
#pragma unroll 1
      for (int ks = 0; ks < 4; ++ks) {
        u32x4 a0, a1, b0, b1;
#pragma unroll
        for (int j = 0; j < 4; ++j) {
          const int n = ks * 16 + g * 4 + j;
          const f32x4 pp = *(const f32x4*)(par + n * 8), pq = *(const f32x4*)(par + n * 8 + 4);
          float tr, ti, xr_, xi_;
          cexp2(__builtin_fmaf(pp[0], va, 14.f), pp[1] * va, tr, ti);
          cexp2(pp[0] * vb, pp[1] * vb, xr_, xi_);
          const float ur = pp[2] * xr_ - pp[3] * xi_, ui = pp[2] * xi_ + pp[3] * xr_;
          a0[j] = pk_f16(tr, ti);
          a1[j] = pk_f16(tr * pq[0] - ti * pq[1], tr * pq[1] + ti * pq[0]);
          b0[j] = pk_f16(ur, -ui);
          b1[j] = pk_f16(ur * pq[2] - ui * pq[3], -(ur * pq[3] + ui * pq[2]));
        }
        acc[0][0] = __builtin_amdgcn_mfma_f32_16x16x32_f16(__builtin_bit_cast(f16x8, a0), __builtin_bit_cast(f16x8, b0), acc[0][0], 0, 0, 0);
        acc[1][0] = __builtin_amdgcn_mfma_f32_16x16x32_f16(__builtin_bit_cast(f16x8, a1), __builtin_bit_cast(f16x8, b0), acc[1][0], 0, 0, 0);
        acc[0][1] = __builtin_amdgcn_mfma_f32_16x16x32_f16(__builtin_bit_cast(f16x8, a0), __builtin_bit_cast(f16x8, b1), acc[0][1], 0, 0, 0);
        acc[1][1] = __builtin_amdgcn_mfma_f32_16x16x32_f16(__builtin_bit_cast(f16x8, a1), __builtin_bit_cast(f16x8, b1), acc[1][1], 0, 0, 0);
      }
      float kl = k2048 * osc;
#pragma unroll
      for (int t = 0; t < 4; ++t) acc[t >> 1][t & 1] *= osc;
      if (zmax > 0.f) {
#pragma unroll
        for (int t = 0; t < 4; ++t)
#pragma unroll
          for (int e = 0; e < 4; ++e)
            acc[t >> 1][t & 1][e] *= __builtin_amdgcn_exp2f(zmax * (vb + (float)(8 * (16 * (t >> 1) + e) + 1024 * (t & 1)) + 32.f * g));
        kl *= __builtin_amdgcn_exp2f(zmax * 2048.f);
      }
      {
        const int stride = r < 8 ? 8 : r < 12 ? 4 : 2;
        const int base = r < 8 ? r : r < 12 ? 256 + (r - 8) : 384 + 64 * ((r - 12) >> 1) + (r & 1);
        float* p0 = tabf + (base + stride * 4 * g) * 2 + hw;
#pragma unroll
        for (int it = 0; it < 2; ++it)
#pragma unroll
          for (int e = 0; e < 4; ++e) p0[stride * (16 * it + e) * 2] = acc[it][0][e];
        if ((off & 255) == 0) {
          float* p1 = tabf + (512 + (off >> 3) + 4 * g) * 2 + hw;
#pragma unroll
          for (int it = 0; it < 2; ++it)
#pragma unroll
            for (int e = 0; e < 4; ++e) p1[(16 * it + e) * 2] = acc[it][1][e];
        }
        if (lane < 2) tabf[(640 + lane) * 2 + hw] = lane ? 0.f : kl;
      }
      grp_arrive(xb, j4, 1, lane);
      const int l2 = hw * 64 + lane;
      char* lk = lds + X_KT + ch * KT_B;
      int ix[17];
#pragma unroll
      for (int e = 0; e < 17; e += 2) {
        const int j0 = 2047 - 16 * l2 - e, j1 = j0 - 1;
        const unsigned bb = pk_bf16((float)j0, (float)j1);
        const int i0 = (int)(bb & 0xffff) - (0x4300 - 128), i1 = (int)(bb >> 16) - (0x4300 - 128);
        ix[e] = j0 < 128 ? (j0 < 0 ? 641 : j0) : i0;
        if (e + 1 < 17) ix[e + 1] = j1 < 128 ? j1 : i1;
      }
      word_wait<1>(sy + 8, 1);
      if (l2 < 8) {
        const int pc = (l2 & 2) ? 258 + (l2 & 1) : (l2 & 1);
        *(u32x4*)(lk + (l2 >> 2) * R1OFF + pc * 16) = u32x4{0, 0, 0, 0};
      }
      if (j4 == 0) *(u32x4*)(lds + X_U + (lane & 1) * U_B + (lane >> 1) * 16) = u32x4{0, 0, 0, 0};
#pragma unroll
      for (int e = 0; e < 17; ++e) asm volatile("" : "+v"(ix[e]));
      grp_wait<0>(xb, 1);
      f32x2 tv[17];
#pragma unroll
      for (int e = 0; e < 17; ++e) tv[e] = *(const f32x2*)(tabf + ix[e] * 2);
      __builtin_amdgcn_sched_barrier(0);
      float val[17];
#pragma unroll
      for (int e = 0; e < 17; ++e) val[e] = tv[e][0] + tv[e][1];
#pragma unroll
      for (int hf = 0; hf < 2; ++hf) {
        u32x4 o0, o1;
#pragma unroll
        for (int e = 0; e < 4; ++e) { o0[e] = pk_bf16(val[8 * hf + 2 * e], val[8 * hf + 2 * e + 1]); o1[e] = pk_bf16(val[8 * hf + 2 * e + 1], val[8 * hf + 2 * e + 2]); }
        *(u32x4*)(lk + 32 + l2 * 32 + hf * 16) = o0;
        *(u32x4*)(lk + R1OFF + 32 + l2 * 32 + hf * 16) = o1;
      }
      if (l2 == 0) *(unsigned*)(lk + R1OFF + 28) = pk_bf16(0.f, val[0]);
    }
    {
      const float dsk = bf_lo(praw[7]);
      const char* lk = lds + X_KT + ch * KT_B;
      const char* lu = lds + X_U + ch * U_B;
      const int col = lane & 15, g = lane >> 4;
      const int p00 = 2047 - col + 8 * g;
      const char* gbase = lk + ((p00 & 1) ? R1OFF : 0) + (p00 >> 1) * 4 - 256 * hw;
      const char* bbase = lu + (UPAD - 16 + 16 * col + 8 * g) * 2 - 256 * hw;
      grp_bar<1>(xb, j4, 2, lane);
      bf16x8 gf[4][8], bf[2][8];
#pragma unroll
      for (int rr = 0; rr < 4; ++rr)
#pragma unroll
        for (int i = 0; i < 8; ++i) {
          const unsigned* gp = (const unsigned*)(gbase - 64 * (8 * i + rr));
          u32x4 gv = {gp[0], gp[1], gp[2], gp[3]};
          gf[rr][i] = __builtin_bit_cast(bf16x8, gv);
        }
#pragma unroll
      for (int rr = 0; rr < 4; ++rr)
        asm volatile("" : "+v"(gf[rr][0]), "+v"(gf[rr][1]), "+v"(gf[rr][2]), "+v"(gf[rr][3]), "+v"(gf[rr][4]), "+v"(gf[rr][5]), "+v"(gf[rr][6]), "+v"(gf[rr][7]));
      unsigned vo2[4];
      w_offsets<L_ * 2>(lane, vo2);
      word_wait(sy + 11, 1);
      if (has1) grp_wait<1>(sy + 20, 1);
      {
        u32x4 u0, u1;
        const int t0 = j4 * 64 + (lane >> 1);
        asm volatile("global_load_dwordx4 %0, %2, off sc1\n\tglobal_load_dwordx4 %1, %3, off sc1\n\ts_waitcnt vmcnt(0)" : "=&v"(u0), "=&v"(u1)
                     : "v"(a.X2 + ((size_t)t0 * 512 + tb * 2 + (lane & 1)) * 16), "v"(a.X2 + ((size_t)(t0 + 32) * 512 + tb * 2 + (lane & 1)) * 16) : "memory");
        char* lw = lds + X_U + (lane & 1) * U_B;
        *(u32x4*)(lw + UPAD * 2 + t0 * 16) = u0;
        *(u32x4*)(lw + UPAD * 2 + (t0 + 32) * 16) = u1;
      }
      grp_arrive(xb, j4, 3, lane);
      if (has1) w_issue<L_ * 2, 3>((const char*)a.w2 + (size_t)(j4 * 32) * (L_ * 2) + sTl * 128, vo2, lbase + j4 * (WL * 4096), 0, XS2);
      if (b == 0 && tid == 256) st4_dev(a.fl, tag);
      grp_wait<0>(xb, 3);
#define BLD(set, rr) do { _Pragma("unroll") for (int i = 0; i < 8; ++i) bf[set][i] = *(const bf16x8*)(bbase + 64 * (8 * i - (rr))); } while (0)
      BLD(0, 0);
      BLD(1, 1);
      f32x4 acc[8];
#pragma unroll
      for (int i = 0; i < 4; ++i) {
        const u32x2 uu = *(const u32x2*)(lu + (UPAD + 256 * (2 * i + hw) + 16 * col + 4 * g) * 2);
        const f32x4 uf = {bf_lo(uu[0]), bf_hi(uu[0]), bf_lo(uu[1]), bf_hi(uu[1])};
        if (hw) { acc[2 * i] = f32x4{0.f, 0.f, 0.f, 0.f}; acc[2 * i + 1] = uf * dsk; } else { acc[2 * i] = uf * dsk; acc[2 * i + 1] = f32x4{0.f, 0.f, 0.f, 0.f}; }
      }
      if (has1) {
        asm volatile("s_waitcnt vmcnt(0)" ::: "memory");
        if (lane == 0) sy[28 + j4] = 1;
        grp_wait<1>(sy + 24, 1);
      }
#pragma unroll
      for (int rr = 0; rr < 4; ++rr) {
#pragma unroll
        for (int bq = 0; bq < 8; ++bq)
#pragma unroll
          for (int i = 0; i + bq < 8; ++i) acc[i + bq] = __builtin_amdgcn_mfma_f32_16x16x32_bf16(gf[rr][bq], bf[rr & 1][i], acc[i + bq], 0, 0, 0);
        asm volatile("" : "+v"(acc[0]), "+v"(acc[1]), "+v"(acc[2]), "+v"(acc[3]), "+v"(acc[4]), "+v"(acc[5]), "+v"(acc[6]), "+v"(acc[7]) :: "memory");
        if (rr + 2 < 4) BLD(rr & 1, rr + 2);
        __builtin_amdgcn_sched_barrier(0);
      }
#undef BLD
      __builtin_amdgcn_s_setprio(3);
      char* xc = lds + X_SC + ch * 8192 + lane * 16;
      if (hw) {
#pragma unroll
        for (int i = 0; i < 4; ++i) { *(f32x4*)(xc + 4096 + i * 1024) = acc[2 * i]; acc[2 * i] = acc[2 * i + 1]; }
      } else {
#pragma unroll
        for (int i = 0; i < 4; ++i) *(f32x4*)(xc + i * 1024) = acc[2 * i + 1];
      }
      grp_arrive(xb, j4, 4, lane);
      word_wait<0>(xb + (j4 ^ 1), 4);
      u32x2 o[4];
#pragma unroll
      for (int i = 0; i < 4; ++i) {
        const f32x4 y = acc[2 * i] + *(const f32x4*)(xc + (hw ^ 1) * 4096 + i * 1024);
        o[i] = u32x2{pk_bf16(y[0], y[1]), pk_bf16(y[2], y[3])};
      }
#pragma unroll
      for (int ip = 0; ip < 2; ++ip) {
        u32x4 q4;
#pragma unroll
        for (int e = 0; e < 2; ++e) {
          auto sw = __builtin_amdgcn_permlane16_swap(o[2 * ip][e], o[2 * ip + 1][e], false, false);
          q4[e] = sw[0]; q4[2 + e] = sw[1];
        }
#pragma unroll
        for (int e = 0; e < 4; ++e) q4[e] = min(q4[e], 0xfffffffeu);
        const int t = 256 * (2 * (2 * ip + (g & 1)) + hw) + 16 * col + 8 * (g >> 1);
        st16_dev(a.sT + ((size_t)d * L_ + t) * 2, q4);
      }
      grp_arrive(xb, j4, 5, lane);
    }
    {
      __builtin_amdgcn_s_setprio(0);
      word_wait<2>(sy + 19, 1);
      grp_wait<1>(xb, 5);
      __builtin_amdgcn_s_setprio(3);
      u32x4 s0, s1;
      const int rw_ = j4 * 16 + (lane >> 3);
      const char* sp = a.sT + ((size_t)(dT2 * 64 + rw_) * L_ + sTl * 64) * 2 + (lane & 7) * 16;
#pragma unroll 1
      for (int att = 0; att < SPIN_CAP; ++att) {
        asm volatile("global_load_dwordx4 %0, %2, off sc1\n\tglobal_load_dwordx4 %1, %3, off sc1\n\ts_waitcnt vmcnt(0)" : "=&v"(s0), "=&v"(s1)
                     : "v"(sp), "v"(sp + (size_t)8 * L_ * 2) : "memory");
        const unsigned mx = max(max(max(s0[0], s0[1]), max(s0[2], s0[3])), max(max(s1[0], s1[1]), max(s1[2], s1[3])));
        if (!__any(mx == ~0u)) break;
        __builtin_amdgcn_s_sleep(8);
      }
      *(u32x4*)(lds + X_SC + rw_ * 144 + (lane & 7) * 16) = s0;
      *(u32x4*)(lds + X_SC + (rw_ + 8) * 144 + (lane & 7) * 16) = s1;
      grp_bar(xb, j4, 6, lane);
      if (tid == 256) sy[10] = 1;
      EpiIn ei;
      epi_load(ei, a.x, a.b2, sTl, dT2, j4, 2, lane);
      epi_load_s(ei, lds + X_SC, j4, 2, lane);
      asm volatile("s_waitcnt vmcnt(0) lgkmcnt(0)" ::: "memory");
      __builtin_amdgcn_s_setprio(0);
      grp_wait<1>(sy, 2);
      __builtin_amdgcn_s_setprio(3);
      const char* ow = lds + j4 * (WL * 4096) + 3 * 4096 + lane * 16;
      epi_finish(ei, *(const f32x4*)ow, *(const f32x4*)(ow + 1024), lds, a.out, j4, 2, lane);
    }
  }
}

__device__ __attribute__((noinline)) void code_end_marker() { asm volatile("s_nop 0"); }

extern "C" void kernel_launch(void* const* d_in, const int* in_sizes, int n_in, void* d_out, int out_size, void* d_ws,
                              size_t ws_size, hipStream_t stream) {
  char* ws = (char*)d_ws;
  Args a;
  a.x = (const u32x4*)d_in[0];
  a.Are = (cus)d_in[1]; a.Aim = (cus)d_in[2]; a.Bre = (cus)d_in[3]; a.Bim = (cus)d_in[4]; a.Cre = (cus)d_in[5]; a.Cim = (cus)d_in[6];
  a.logdt = (cus)d_in[7]; a.Dskip = (cus)d_in[8];
  a.w1 = (cus)d_in[10]; a.b1 = (cus)d_in[11]; a.w2 = (cus)d_in[12]; a.b2 = (cus)d_in[13];
  a.out = (unsigned short*)d_out;
  a.X2 = ws; a.Hf = ws + (2u << 20); a.Gf = ws + (4u << 20); a.sT = ws + (6u << 20); a.fl = (unsigned*)(ws + (8u << 20));
  (void)hipFuncSetAttribute((const void*)k_mega, hipFuncAttributeMaxDynamicSharedMemorySize, LDS_B);
  k_mega<<<256, 512, LDS_B, stream>>>(a);
}
```

```cpp
#include <hip/hip_runtime.h>
#include <hip/hip_bf16.h>
#include <stdint.h>

typedef short s16x4 __attribute__((ext_vector_type(4)));
typedef short bf16x8 __attribute__((ext_vector_type(8)));
typedef _Float16 f16x8 __attribute__((ext_vector_type(8)));
typedef _Float16 h2_t __attribute__((ext_vector_type(2)));
typedef __bf16 bf2_t __attribute__((ext_vector_type(2)));
typedef float f32x2 __attribute__((ext_vector_type(2)));
typedef float f32x4 __attribute__((ext_vector_type(4)));
typedef unsigned u32x2 __attribute__((ext_vector_type(2)));
typedef unsigned u32x4 __attribute__((ext_vector_type(4)));
typedef const unsigned short* cus;
#define DI __device__ __forceinline__
#define LDSP __attribute__((address_space(3)))


#define RW1 5
#define RW2 5
#define WL 7
#define AD 3
#define XS2 4
#define PBK 6
#define HQ 16
#define GSP 32
#define GSTEP (16 - AD)
#define PSTEP 9
#define IDEL 800

constexpr int L_ = 2048, D_ = 512, N_ = 128, F_ = 1792;
constexpr int TABN = 648;
constexpr float LOG2E = 1.44269504088896341f;
constexpr int SPIN_CAP = 1 << 18;

DI unsigned pk_bf16(float lo, float hi) {
  f32x2 f = {lo, hi};
  return __builtin_bit_cast(unsigned, __builtin_convertvector(f, bf2_t));
}
DI unsigned pk_f16(float lo, float hi) {
  f32x2 f = {lo, hi};
  return __builtin_bit_cast(unsigned, __builtin_convertvector(f, h2_t));
}
DI float bf_lo(unsigned u) { return __uint_as_float(u << 16); }
DI float bf_hi(unsigned u) { return __uint_as_float(u & 0xffff0000u); }
DI float rbf(float x) { return bf_lo(pk_bf16(x, 0.f)); }

template <int CTRL, int RM, bool BC> DI float dppf(float v) {
  return __builtin_bit_cast(float, __builtin_amdgcn_update_dpp(0, __builtin_bit_cast(int, v), CTRL, RM, 0xf, BC));
}
DI float wave_sum(float v) {
  v += dppf<0x111, 0xf, true>(v);
  v += dppf<0x112, 0xf, true>(v);
  v += dppf<0x114, 0xf, true>(v);
  v += dppf<0x118, 0xf, true>(v);
  v += dppf<0x142, 0xa, false>(v);
  v += dppf<0x143, 0xc, false>(v);
  return __builtin_bit_cast(float, __builtin_amdgcn_readlane(__builtin_bit_cast(int, v), 63));
}
DI float wave_max_nn(float v) {
  v = fmaxf(v, dppf<0x111, 0xf, true>(v));
  v = fmaxf(v, dppf<0x112, 0xf, true>(v));
  v = fmaxf(v, dppf<0x114, 0xf, true>(v));
  v = fmaxf(v, dppf<0x118, 0xf, true>(v));
  v = fmaxf(v, dppf<0x142, 0xa, false>(v));
  v = fmaxf(v, dppf<0x143, 0xc, false>(v));
  return __builtin_bit_cast(float, __builtin_amdgcn_readlane(__builtin_bit_cast(int, v), 63));
}
DI void touch(unsigned ldum, const void* p) { asm volatile("s_mov_b32 m0, %0\n\ts_nop 0\n\tglobal_load_lds_dword %1, off" :: "s"(ldum), "v"(p) : "memory"); }
DI void lds_barrier() { asm volatile("s_waitcnt lgkmcnt(0)" ::: "memory"); __builtin_amdgcn_s_barrier(); asm volatile("" ::: "memory"); }
DI void st16_dev(void* p, u32x4 v) { asm volatile("global_store_dwordx4 %0, %1, off sc1\n\ts_nop 1" :: "v"(p), "v"(v) : "memory"); }
DI void st16_dev_s(const void* sb, unsigned vo, u32x4 v) { asm volatile("global_store_dwordx4 %0, %1, %2 sc1\n\ts_nop 1" :: "v"(vo), "v"(v), "s"(sb) : "memory"); }
DI void st4_dev_s(const void* sb, unsigned v) { asm volatile("global_store_dword %0, %1, %2 sc1" :: "v"(0), "v"(v), "s"(sb) : "memory"); }
DI void st4_dev(void* p, unsigned v) { asm volatile("global_store_dword %0, %1, off sc1" :: "v"(p), "v"(v) : "memory"); }
DI unsigned ld4_dev_wait(const void* p) { unsigned r; asm volatile("global_load_dword %0, %1, off sc1\n\ts_waitcnt vmcnt(0)" : "=&v"(r) : "v"(p) : "memory"); return r; }

DI void grp_arrive(volatile LDSP unsigned* words, int idx, unsigned ph, int lane) {
  asm volatile("s_waitcnt lgkmcnt(0)" ::: "memory");
  if (lane == 0) words[idx] = ph;
}
template <int SL = 1>
DI void grp_wait(volatile LDSP unsigned* words, unsigned ph) {
#pragma unroll 1
  for (int sp = 0; sp < SPIN_CAP * 8; ++sp) {
    const u32x4 v = *(volatile LDSP u32x4*)words;
    const unsigned m = min(min(v[0], v[1]), min(v[2], v[3]));
    if (__builtin_amdgcn_readfirstlane(m) >= ph) break;
    if (SL) __builtin_amdgcn_s_sleep(SL);
  }
  asm volatile("" ::: "memory");
}
template <int SL = 1>
DI void grp_bar(volatile LDSP unsigned* words, int idx, unsigned ph, int lane) { grp_arrive(words, idx, ph, lane); grp_wait<SL>(words, ph); }
template <int SL = 1>
DI void word_wait(volatile LDSP unsigned* word, unsigned v) {
#pragma unroll 1
  for (int sp = 0; sp < SPIN_CAP * 8; ++sp) {
    if (__builtin_amdgcn_readfirstlane(*word) >= v) break;
    if (SL) __builtin_amdgcn_s_sleep(SL);
  }
  asm volatile("" ::: "memory");
}
template <int BK = PBK>
DI void poll_flags(const unsigned* p, unsigned tag, bool done) {
#pragma unroll 1
  for (int sp = 0; sp < SPIN_CAP; ++sp) {
    if (!done) done = ld4_dev_wait(p) == tag;
    if (__all(done)) break;
    __builtin_amdgcn_s_sleep(BK);
  }
}
DI void poll_flags4(const unsigned* p, unsigned tag) {
  bool done = false;
#pragma unroll 1
  for (int sp = 0; sp < SPIN_CAP; ++sp) {
    if (!done) {
      u32x4 f;
      asm volatile("global_load_dwordx4 %0, %1, off sc1\n\ts_waitcnt vmcnt(0)" : "=&v"(f) : "v"(p) : "memory");
      done = f[0] == tag && f[1] == tag && f[2] == tag && f[3] == tag;
    }
    if (__all(done)) break;
    __builtin_amdgcn_s_sleep(PBK);
  }
}

DI int kpos(int kb) { return kb ^ (((kb >> 2) & 1) << 1); }

template <bool PIVOT>
DI void ln8(const u32x4& in, u32x4& o) {
  f32x2 v[4];
#pragma unroll
  for (int i = 0; i < 4; ++i) v[i] = f32x2{bf_lo(in[i]), bf_hi(in[i])};
  const float p = PIVOT ? __builtin_bit_cast(float, __builtin_amdgcn_readfirstlane(__builtin_bit_cast(int, v[0][0]))) : 0.f;
  f32x2 t[4];
#pragma unroll
  for (int i = 0; i < 4; ++i) t[i] = PIVOT ? v[i] - p : v[i];
  const f32x2 s2 = (t[0] + t[1]) + (t[2] + t[3]);
  const f32x2 q2 = __builtin_elementwise_fma(t[0], t[0], t[1] * t[1]) + __builtin_elementwise_fma(t[2], t[2], t[3] * t[3]);
  const float ds = wave_sum(s2[0] + s2[1]) * (1.f / D_), dq = wave_sum(q2[0] + q2[1]) * (1.f / D_);
  const float mean = p + ds, var = fmaxf(__builtin_fmaf(-ds, ds, dq), 0.f);
  const float mb = rbf(mean), vb = rbf(var);
  const float den = rbf(__builtin_amdgcn_sqrtf(rbf(vb + 1e-5f)));
  const float inv = __builtin_amdgcn_rcpf(den);
#pragma unroll
  for (int i = 0; i < 4; ++i) {
    const f32x2 c = v[i] - mb;
    const unsigned r = pk_bf16(c[0], c[1]);
    const f32x2 n = f32x2{bf_lo(r), bf_hi(r)} * inv;
    o[i] = pk_bf16(n[0], n[1]);
  }
}
DI void cexp2(float le, float ph, float& re, float& im) {
  const float e = __builtin_amdgcn_exp2f(le);
  const float p = __builtin_amdgcn_fractf(ph);
  re = e * __builtin_amdgcn_cosf(p);
  im = e * __builtin_amdgcn_sinf(p);
}
DI f32x2 xsig2(f32x2 x, f32x2 e) {
  const f32x2 d = f32x2{__builtin_amdgcn_exp2f(e[0]), __builtin_amdgcn_exp2f(e[1])} + 1.f;
  return x * f32x2{__builtin_amdgcn_rcpf(d[0]), __builtin_amdgcn_rcpf(d[1])};
}
DI f32x2 gelu_tanh2(f32x2 x) {
  constexpr float k1 = -2.f * 0.7978845608028654f * LOG2E, k2 = k1 * 0.044715f;
  return xsig2(x, x * __builtin_elementwise_fma(x * x, f32x2{k2, k2}, f32x2{k1, k1}));
}
DI f32x2 silu2(f32x2 x) { return xsig2(x, x * -LOG2E); }
DI f32x2 bf_pair(unsigned u) { return f32x2{bf_lo(u), bf_hi(u)}; }

#define DMA4(ldsaddr, vo, sb) do { unsigned keep_; \
    asm volatile("s_mov_b32 %0, m0\n\ts_mov_b32 m0, %1\n\ts_nop 0\n\t" \
                 "global_load_lds_dwordx4 %2, %6\n\tglobal_load_lds_dwordx4 %3, %6 offset:1024\n\t" \
                 "global_load_lds_dwordx4 %4, %6 offset:2048\n\tglobal_load_lds_dwordx4 %5, %6 offset:3072\n\ts_mov_b32 m0, %0" \
                 : "=&s"(keep_) : "s"(__builtin_amdgcn_readfirstlane(ldsaddr)), "v"(vo[0]), "v"(vo[1]), "v"(vo[2]), "v"(vo[3]), "s"(sb) : "memory"); } while (0)
template <int RB> DI void w_offsets(int lane, unsigned (&vo)[4]) {
  const int lr = lane >> 3, p8 = lane & 7;
#pragma unroll
  for (int j = 0; j < 4; ++j) vo[j] = (8 * j + lr) * RB + ((p8 ^ ((lr & 2) | ((j & 1) << 2))) << 4) - 1024 * j;
}
struct TrAddr { const LDSP char* p[4]; };
DI void tr_addr(TrAddr& t, const LDSP char* ring, int lane) {
  const int g = lane >> 4, q = (lane & 15) >> 2, p = lane & 3;
  const int frow = (q & 2) | ((g & 1) << 2);
  const LDSP char* trb = ring + (8 * g + q) * 128 + (p & 1) * 8;
#pragma unroll
  for (int j = 0; j < 4; ++j) t.p[j] = trb + (((2 * j + (p >> 1)) ^ frow) << 4);
}
DI void tr_read(bf16x8 (&fr)[4], const TrAddr& t, int off) {
#pragma unroll
  for (int j = 0; j < 4; ++j) {
    s16x4 lo = __builtin_amdgcn_ds_read_tr16_b64_v4i16((LDSP s16x4*)(t.p[j] + off));
    s16x4 hi = __builtin_amdgcn_ds_read_tr16_b64_v4i16((LDSP s16x4*)(t.p[j] + off + 512));
    fr[j] = __builtin_shufflevector(lo, hi, 0, 1, 2, 3, 4, 5, 6, 7);
  }
}
template <int RB, int OFF>
DI void w_issue(const char* wb, const unsigned (&vo)[4], unsigned lring, int k0, int k1) {
#pragma unroll 1
  for (int k = k0; k < k1; ++k) DMA4(lring + ((k + OFF) % WL) * 4096, vo, wb + (size_t)k * 128 * RB);
}
template <int RWN, int OFF, int K0 = 0, int K1 = RWN>
DI void w_to_regs(const TrAddr& tra, bf16x8 (&wr)[RWN][4]) {
#pragma unroll
  for (int k = K0; k < K1; ++k) tr_read(wr[k], tra, ((k + OFF) % WL) * 4096);
#pragma unroll
  for (int k = K0; k < K1; ++k) asm volatile("s_waitcnt lgkmcnt(0)" : "+v"(wr[k][0]), "+v"(wr[k][1]), "+v"(wr[k][2]), "+v"(wr[k][3]) :: "memory");
}
template <int NKB, int RWN, int OFF> struct Sch {
  static constexpr int NL = NKB - RWN;
  static constexpr int blk(int i) { return i < NL ? RWN + i : i - NL; }
  static constexpr int slot(int i) { return (RWN + i + OFF) % WL; }
  static constexpr bool dma(int s) { return s >= 1 && s - 1 + WL < NL; }
  static constexpr int younger(int i) {
    int pos = 0, pa = -1, pw = -1;
    for (int k = 0; k < AD; ++k) { if (k == i) pa = pos; ++pos; }
    for (int s = 0; s < i; ++s) {
      if (dma(s)) { if (s - 1 + WL == i + 1) pw = pos; ++pos; }
      if (s + AD < NKB) { if (s + AD == i) pa = pos; ++pos; }
    }
    const int need = pa > pw ? pa : pw;
    return pos - 1 - need;
  }
};
DI bf16x8 tr_frag(const LDSP char* p) {
  s16x4 lo = __builtin_amdgcn_ds_read_tr16_b64_v4i16((LDSP s16x4*)(p));
  s16x4 hi = __builtin_amdgcn_ds_read_tr16_b64_v4i16((LDSP s16x4*)(p + 512));
  return __builtin_shufflevector(lo, hi, 0, 1, 2, 3, 4, 5, 6, 7);
}
template <int NKB, int RB, bool SWAP, int RWN, int OFF>
DI void gemm_main(const char* ab, const char* abo, const char* wb, const unsigned (&vo)[4], unsigned lring, const TrAddr& tra,
                  bf16x8 (&wr)[RWN][4], int w, int lane, f32x4 (&acc)[4][4], char* park, volatile LDSP unsigned* sig) {
  typedef Sch<NKB, RWN, OFF> S;
  unsigned va[4];
#pragma unroll
  for (int t = 0; t < 4; ++t) va[t] = lane * 16 + ((t + w + 1) & 3) * 1024;
  u32x4 av[AD][4];
  bf16x8 fr[4];
#define ABASE(i) (((S::blk(i) & 1) ? abo : ab) + (size_t)S::blk(i) * 16384)
#define LDA1(i, t) asm volatile("global_load_dwordx4 %0, %1, %2 sc1" : "=&v"(av[(i) % AD][t]) : "v"(va[t]), "s"(ABASE(i)) : "memory")
#define DMA1(i, q) asm volatile("s_mov_b32 m0, %0\n\ts_nop 0\n\tglobal_load_lds_dwordx4 %1, %2 offset:%3" \
                 :: "s"(__builtin_amdgcn_readfirstlane(lring + S::slot((i) - 1) * 4096)), "v"(vo[q]), "s"(wb + (size_t)(RWN + (i) - 1 + WL) * 128 * RB), "i"((q) * 1024) : "memory")
#pragma unroll
  for (int i = 0; i < AD; ++i) { LDA1(i, 0); LDA1(i, 1); LDA1(i, 2); LDA1(i, 3); }
  asm volatile("s_waitcnt vmcnt(%0)" :: "i"(4 * AD) : "memory");
#pragma unroll
  for (int j = 0; j < 4; ++j) fr[j] = tr_frag(tra.p[j] + S::slot(0) * 4096);
#pragma unroll
  for (int i = 0; i < NKB; ++i) {
    const int st = i % AD;
    asm volatile("s_waitcnt vmcnt(%4)" : "+v"(av[st][0]), "+v"(av[st][1]), "+v"(av[st][2]), "+v"(av[st][3]) : "i"(4 * S::younger(i)) : "memory");
    if (i == (SWAP ? PSTEP : GSTEP) && lane == 0) *sig = 1;
    if (i == NKB - 1) {
#pragma unroll
      for (int m = 0; m < 16; ++m) {
        const int t = m >> 2, j = m & 3;
        if (SWAP) asm volatile("v_mfma_f32_16x16x32_bf16 %0, %1, %2, %0" : "+v"(acc[j][t]) : "v"(av[st][t]), "v"(wr[RWN - 1][j]) : "memory");
        else asm volatile("v_mfma_f32_16x16x32_bf16 %0, %1, %2, %0" : "+v"(acc[j][t]) : "v"(wr[RWN - 1][j]), "v"(av[st][t]) : "memory");
        if (m >= 8) *(f32x4*)(park + (m - 8) * 1024) = acc[(m - 8) & 3][(m - 8) >> 2];
      }
      continue;
    }
#pragma unroll
    for (int j = 0; j < 4; ++j) {
#pragma unroll
      for (int t = 0; t < 4; ++t) {
        const bf16x8 wf = i < S::NL ? fr[j] : wr[i < S::NL ? 0 : i - S::NL][j];
        if (i == 0) {
          if (SWAP) asm volatile("v_mfma_f32_16x16x32_bf16 %0, %1, %2, 0" : "=&v"(acc[j][t]) : "v"(av[st][t]), "v"(wf) : "memory");
          else asm volatile("v_mfma_f32_16x16x32_bf16 %0, %1, %2, 0" : "=&v"(acc[j][t]) : "v"(wf), "v"(av[st][t]) : "memory");
        } else {
          if (SWAP) asm volatile("v_mfma_f32_16x16x32_bf16 %0, %1, %2, %0" : "+v"(acc[j][t]) : "v"(av[st][t]), "v"(wf) : "memory");
          else asm volatile("v_mfma_f32_16x16x32_bf16 %0, %1, %2, %0" : "+v"(acc[j][t]) : "v"(wf), "v"(av[st][t]) : "memory");
        }
        if (S::dma(i) && j < 2 && (t == 0 || t == 2)) { if (j == 0 && t == 0) DMA1(i, 0); else if (j == 0) DMA1(i, 1); else if (t == 0) DMA1(i, 2); else DMA1(i, 3); }
        if (j == 3 && i + AD < NKB) { if (t == 0) LDA1(i + AD, 0); else if (t == 1) LDA1(i + AD, 1); else if (t == 2) LDA1(i + AD, 2); else LDA1(i + AD, 3); }
      }
      if (i + 1 < S::NL) fr[j] = tr_frag(tra.p[j] + S::slot(i + 1) * 4096);
    }
  }
#pragma unroll
  for (int j = 0; j < 4; ++j) { *(f32x4*)(park + (8 + j) * 1024) = acc[j][2]; asm volatile("" ::: "memory"); }
  asm volatile("s_nop 7\n\ts_nop 7" ::: "memory");
#undef LDA1
#undef DMA1
#undef ABASE
}
DI void reduce4(f32x4 (&acc)[4][4], f32x4 (&res)[4], char* lds, int w, int lane, volatile LDSP unsigned* words, unsigned ph) {
  grp_bar<0>(words, w, ph, lane);
  f32x4 pv[3][4];
#pragma unroll
  for (int k = 1; k < 4; ++k) {
    const char* p = lds + ((w + k) & 3) * (WL * 4096) + (3 - k) * 4096 + lane * 16;
#pragma unroll
    for (int j = 0; j < 4; ++j) pv[k - 1][j] = *(const f32x4*)(p + j * 1024);
  }
#pragma unroll
  for (int j = 0; j < 4; ++j) res[j] = (acc[j][3] + pv[0][j]) + (pv[1][j] + pv[2][j]);
}

struct EpiIn { u32x2 xv[2], sv[2]; unsigned braw[2]; size_t oo; };
DI void epi_load(EpiIn& e, const u32x4* x, cus b2, int sTl, int dT2, int w, int j0, int lane) {
  const int g = lane >> 4, c = lane & 15;
  e.oo = (size_t)(sTl * 64 + 16 * j0 + c) * D_ + dT2 * 64 + 16 * w + 4 * g;
#pragma unroll
  for (int j = 0; j < 2; ++j) {
    e.braw[j] = b2[sTl * 64 + 16 * (j0 + j) + c];
    e.xv[j] = *(const u32x2*)((cus)x + e.oo + (size_t)(16 * j) * D_);
  }
}
DI void epi_load_s(EpiIn& e, const char* st, int w, int j0, int lane) {
#pragma unroll
  for (int j = 0; j < 2; ++j)
    e.sv[j] = __builtin_bit_cast(u32x2, __builtin_amdgcn_ds_read_tr16_b64_v4i16((LDSP s16x4*)(st + (16 * w + (lane >> 2)) * 144 + (j0 + j) * 32 + (lane & 3) * 8)));
}
DI void epi_finish(const EpiIn& e, f32x4 own0, f32x4 own1, const char* lds, unsigned short* out, int w, int j0, int lane) {
  f32x4 pv[3][2];
#pragma unroll
  for (int k = 1; k < 4; ++k) {
    const char* p = lds + ((w + k) & 3) * (WL * 4096) + (3 - k) * 4096 + j0 * 1024 + lane * 16;
#pragma unroll
    for (int j = 0; j < 2; ++j) pv[k - 1][j] = *(const f32x4*)(p + j * 1024);
  }
#pragma unroll
  for (int j = 0; j < 2; ++j) {
    const f32x4 r = ((j ? own1 : own0) + pv[0][j]) + (pv[1][j] + pv[2][j]) + bf_lo(e.braw[j]);
    const f32x2 o0 = __builtin_elementwise_fma(bf_pair(e.sv[j][0]), silu2(f32x2{r[0], r[1]}), bf_pair(e.xv[j][0]));
    const f32x2 o1 = __builtin_elementwise_fma(bf_pair(e.sv[j][1]), silu2(f32x2{r[2], r[3]}), bf_pair(e.xv[j][1]));
    *(u32x2*)(out + e.oo + (size_t)(16 * j) * D_) = u32x2{pk_bf16(o0[0], o0[1]), pk_bf16(o1[0], o1[1])};
  }
}

struct Args {
  const u32x4* x; cus Are, Aim, Bre, Bim, Cre, Cim, logdt, Dskip, w1, b1, w2, b2;
  unsigned short* out;
  char* Hf;  char* X2; char* Gf; char* sT; unsigned* fl;
};
constexpr int FL_H = 64, FL_G = 2048;

constexpr int KR = 2080;
constexpr int UPAD = 256;
constexpr int R1OFF = KR * 2 + 128;
constexpr int KT_B = R1OFF + KR * 2 + 32;
constexpr int U_B = (UPAD + L_) * 2;
constexpr int XR = 4 * WL * 4096;
constexpr int X_KT = XR;
constexpr int X_U = X_KT + 2 * KT_B;
constexpr int X_SC = X_U + 2 * U_B;
constexpr int SC_B = 8192 + 2 * TABN * 8;
constexpr int X_SY = X_SC + SC_B;
constexpr int X_B1 = X_SY + 128;
constexpr int X_DUM = X_B1 + 512;
constexpr int LDS_B = X_DUM + 256;
static_assert(LDS_B <= 160 * 1024 && SC_B >= 16384 && 2 * KT_B >= 16 * (1024 + 32), "LDS");

__device__ __attribute__((noinline)) void code_end_marker();

__global__ __launch_bounds__(512, 2) void k_mega(Args a) {
  extern __shared__ __attribute__((aligned(16))) char lds[];
  const int tid = threadIdx.x, lane = tid & 63, w = __builtin_amdgcn_readfirstlane(tid >> 6);
  const int b = blockIdx.x, xg = b & 7, r_ = b >> 3;
  const int tb = xg * 32 + r_;
  const unsigned lbase = (unsigned)(size_t)(LDSP char*)lds;
  volatile LDSP unsigned* sy = (volatile LDSP unsigned*)(lds + X_SY);
  const bool has1 = r_ < 28;
  const int idx1 = xg * 28 + (has1 ? r_ : 0);
  const int fT = idx1 >> 3, dT = idx1 & 7;
  const int sTl = tb >> 3, dT2 = tb & 7;
  u32x4 xr; unsigned ep;
  asm volatile("global_load_dwordx4 %0, %2, %3\n\tglobal_load_dword %1, %4, %5 sc1" : "=&v"(xr), "=&v"(ep)
               : "v"(lane * 16), "s"(a.x + (size_t)(tb * 8 + w) * 64), "v"(0), "s"(a.fl) : "memory");
  if (tid < 32) sy[tid] = 0;
  constexpr int RS = 1024 + 32;
  const LDSP char* lnp = (const LDSP char*)lds + X_KT + ((lane & 15) >> 2) * RS + ((w & 3) * 8 + (lane >> 4)) * 32 + (lane & 3) * 8;
  unsigned hvo = (lane >> 4) * 1024 + (lane & 15) * 16;
  const char* hsb = a.Hf + (size_t)(2 * (w & 3) * 64 + kpos(tb >> 2)) * 4096 + (tb & 3) * 256;
  const char* xsb = a.X2 + (size_t)tb * 8192 + (w & 3) * 2048;
  asm volatile("" : "+v"(lnp), "+v"(hvo));
  asm volatile("" : "+s"(hsb), "+s"(xsb));
  unsigned praw[8];
  if (w >= 4) {
    const int i = (tb * 2) * N_ + (w - 4) * 64;
    const int dch = tb * 2 + ((w - 4) >> 1);
    asm volatile("global_load_ushort %0, %8, %10\n\tglobal_load_ushort %1, %8, %11\n\tglobal_load_ushort %2, %8, %12\n\tglobal_load_ushort %3, %8, %13\n\t"
                 "global_load_ushort %4, %8, %14\n\tglobal_load_ushort %5, %8, %15\n\tglobal_load_ushort %6, %9, %16\n\tglobal_load_ushort %7, %9, %17\n\ts_barrier"
                 : "=&v"(praw[0]), "=&v"(praw[1]), "=&v"(praw[2]), "=&v"(praw[3]), "=&v"(praw[4]), "=&v"(praw[5]), "=&v"(praw[6]), "=&v"(praw[7])
                 : "v"(lane * 2), "v"(0), "s"(a.Are + i), "s"(a.Aim + i), "s"(a.Bre + i), "s"(a.Bim + i), "s"(a.Cre + i), "s"(a.Cim + i),
                   "s"(a.logdt + dch), "s"(a.Dskip + dch) : "memory");
    const int li = r_ * 256 + tid - 256, ft0 = (xg * 28) >> 3;
    const char* t1 = (const char*)a.w1 + (size_t)(li >> 2) * (F_ * 2) + (ft0 + (li & 3)) * 128;
    const char* t2 = t1;
    {
      unsigned long long pc;
      asm volatile("s_getpc_b64 %0" : "=s"(pc));
      const unsigned long long pe = (unsigned long long)(void*)&code_end_marker;
      const unsigned long long pa = (pc & ~127ull) + (unsigned long long)(((w - 4) * 4 + (r_ & 3)) * 64 + lane) * 128;
      if (pe > pc && pe - pc < 65536 && pa + 4 <= pe && r_ >= 28 && w < 6) t2 = (const char*)pa;
    }
    asm volatile("s_mov_b32 m0, %2\n\ts_nop 0\n\tglobal_load_lds_dword %1, off\n\tglobal_load_lds_dword %0, off\n\ts_waitcnt vmcnt(10)"
                 :: "v"(t1), "v"(t2), "s"(lbase + X_DUM) : "memory");
  } else {
    u32x4 bv;
    const u32x4 mark = {~0u, ~0u, ~0u, ~0u};
    asm volatile("global_load_dwordx4 %0, %1, %2\n\ts_barrier\n\tglobal_store_dwordx4 %3, %4, %5 sc1\n\tglobal_store_dwordx4 %3, %4, %5 offset:1024 sc1\n\t"
                 "s_waitcnt vmcnt(2)"
                 : "=&v"(bv) : "v"((lane & 7) * 16), "s"(a.b1 + fT * 64), "v"(lane * 16), "v"(mark), "s"(a.sT + (size_t)tb * 8192 + w * 2048) : "memory");
    if (lane < 8) *(u32x4*)(lds + X_B1 + w * 128 + lane * 16) = bv;
  }
  asm volatile("" : "+v"(xr), "+v"(ep));
  const unsigned tag = __builtin_amdgcn_readfirstlane(ep) * 0x9E3779B1u + 0x7F4A7C15u;
  {
    u32x4 o1, o2;
    ln8<true>(xr, o1);
    *(u32x4*)(lds + X_KT + w * RS + lane * 16) = o1;
    ln8<false>(o1, o2);
    *(u32x4*)(lds + X_KT + (8 + w) * RS + lane * 16) = o2;
  }
  lds_barrier();

  if (__builtin_expect(w < 4, 1)) {
    __builtin_amdgcn_s_setprio(3);
    const int g = lane >> 4, c = lane & 15;
    {
      u32x4 ov[2][2];
#pragma unroll
      for (int arr = 1; arr >= 0; --arr)
#pragma unroll
        for (int it = 0; it < 2; ++it) {
          const LDSP char* base = lnp + arr * 8 * RS + it * 128;
          s16x4 lo = __builtin_amdgcn_ds_read_tr16_b64_v4i16((LDSP s16x4*)(base));
          s16x4 hi = __builtin_amdgcn_ds_read_tr16_b64_v4i16((LDSP s16x4*)(base + 4 * RS));
          ov[arr][it] = __builtin_bit_cast(u32x4, __builtin_shufflevector(lo, hi, 0, 1, 2, 3, 4, 5, 6, 7));
        }
#pragma unroll
      for (int it = 0; it < 2; ++it) st16_dev_s(hsb + it * (64 * 4096), hvo, ov[1][it]);
#pragma unroll
      for (int it = 0; it < 2; ++it) st16_dev_s(xsb + it * 1024, lane * 16, ov[0][it]);
    }
    const unsigned lring = lbase + w * (WL * 4096);
    TrAddr tra;
    tr_addr(tra, (const LDSP char*)lds + w * (WL * 4096), lane);
    f32x4 acc[4][4], res[4];
    unsigned vo[4];
    w_offsets<F_ * 2>(lane, vo);
    const char* wb1 = (const char*)a.w1 + (size_t)(w * 32) * (F_ * 2) + fT * 128;
    const char* wb2 = (const char*)a.w2 + (size_t)(w * 32) * (L_ * 2) + sTl * 128;
    unsigned long long tH = 0;
    bf16x8 wr2[RW2][4];
    const unsigned* flh = a.fl + FL_H + (tb >> 2) * HQ + (tb & 3);
    asm volatile("" : "+s"(wb1), "+s"(flh));
    asm volatile("s_waitcnt vmcnt(0)" : "+v"(vo[0]), "+v"(vo[1]), "+v"(vo[2]), "+v"(vo[3]), "+v"(tra.p[0]), "+v"(tra.p[1]), "+v"(tra.p[2]), "+v"(tra.p[3]) :: "memory");
    grp_bar<0>(sy + 12, w, 1, lane);
    if (tid == 0) st4_dev_s(flh, tag);
    if (!has1) __builtin_amdgcn_s_setprio(0);
    if (__builtin_expect(has1, 1)) {
      static_assert(GSTEP >= 16 - RW1, "the ring must be drained before it is handed over");
      bf16x8 wr1[RW1][4];
      w_issue<F_ * 2, 0>(wb1, vo, lring, 0, WL);
      asm volatile("s_waitcnt vmcnt(%0)" :: "i"(4 * (WL - RW1)) : "memory");
      w_to_regs<RW1, 0>(tra, wr1);
      w_issue<F_ * 2, 0>(wb1, vo, lring, WL, WL + RW1);
      if (w == 0) {
        poll_flags4(a.fl + FL_H + lane * HQ, tag);
        if (lane == 0) sy[11] = 1;
      } else word_wait<1>(sy + 11, 1);
      gemm_main<16, F_ * 2, false, RW1, 0>(a.Hf + (size_t)(dT * 64 + w) * 4096, a.Hf + (size_t)(dT * 64 + (w ^ 2)) * 4096, wb1, vo, lring, tra, wr1, w, lane, acc,
                                           lds + w * (WL * 4096) + lane * 16, sy + 20 + w);
      reduce4(acc, res, lds, w, lane, sy, 1);
      u32x2 o[4];
#pragma unroll
      for (int j = 0; j < 4; ++j) {
        const u32x2 bb = *(const u32x2*)(lds + X_B1 + w * 128 + (16 * j + 4 * g) * 2);
        const f32x2 v0 = gelu_tanh2(f32x2{res[j][0], res[j][1]} + bf_pair(bb[0])), v1 = gelu_tanh2(f32x2{res[j][2], res[j][3]} + bf_pair(bb[1]));
        o[j] = u32x2{pk_bf16(v0[0], v0[1]), pk_bf16(v1[0], v1[1])};
      }
#pragma unroll
      for (int jp = 0; jp < 2; ++jp) {
        u32x4 q4;
#pragma unroll
        for (int e = 0; e < 2; ++e) {
          auto sw = __builtin_amdgcn_permlane16_swap(o[2 * jp][e], o[2 * jp + 1][e], false, false);
          q4[e] = sw[0]; q4[2 + e] = sw[1];
        }
        st16_dev_s(a.Gf + ((size_t)(dT * 56 + kpos(2 * fT + jp)) * 4 + w) * 1024, ((2 * (g & 1) + (g >> 1)) * 16 + c) * 16, q4);
      }
      grp_arrive(sy + 24, w, 1, lane);
      w_offsets<L_ * 2>(lane, vo);
      const unsigned* flg = a.fl + FL_G + (dT * 32 + fT) * GSP;
      asm volatile("" : "+s"(flg));
      asm volatile("s_waitcnt vmcnt(0)" : "+v"(vo[0]), "+v"(vo[1]), "+v"(vo[2]), "+v"(vo[3]) :: "memory");
      grp_bar<0>(sy + 12, w, 2, lane);
      if (tid == 0) st4_dev_s(flg, tag);
      w_issue<L_ * 2, 3>(wb2, vo, lring, XS2, WL);
      word_wait<0>(sy + 28 + w, 1);
      w_to_regs<RW2, 3, 0, XS2>(tra, wr2);
    } else {
      const int bq = r_ - 28;
      tH = __builtin_amdgcn_s_memrealtime();
#pragma unroll 1
      for (int li = tid; li < 1792; li += 256) {
        touch(lbase + X_DUM, (const char*)a.w2 + (size_t)((li >> 2) * 4 + bq) * (L_ * 2) + (xg * 4 + (li & 3)) * 128);
        asm volatile("s_waitcnt vmcnt(%0)" :: "i"(3) : "memory");
      }
      asm volatile("s_waitcnt vmcnt(0)" ::: "memory");
      grp_bar<2>(sy + 12, w, 2, lane);
      if (w == 0) {
        poll_flags4(a.fl + FL_H + lane * HQ, tag);
        if (lane == 0) sy[11] = 1;
      }
      grp_wait<2>(sy + 4, 3);
      w_offsets<L_ * 2>(lane, vo);
      w_issue<L_ * 2, 3>(wb2, vo, lring, 0, WL);
      asm volatile("s_waitcnt vmcnt(0)" ::: "memory");
      w_to_regs<RW2, 3, 0, XS2>(tra, wr2);
    }
    static_assert(XS2 <= WL - 3 && RW2 >= XS2 && RW2 <= WL, "");
    w_issue<L_ * 2, 3>(wb2, vo, lring, WL, XS2 + WL);
    if (RW2 > XS2) {
      asm volatile("s_waitcnt vmcnt(%0)" :: "i"(4 * (XS2 + WL - RW2)) : "memory");
      w_to_regs<RW2, 3, XS2, RW2>(tra, wr2);
      w_issue<L_ * 2, 3>(wb2, vo, lring, XS2 + WL, RW2 + WL);
    }
    if (w == 0) {
      if (!has1) {
#pragma unroll 1
        for (int sp = 0; sp < 4096 && __builtin_amdgcn_s_memrealtime() - tH < IDEL; ++sp) __builtin_amdgcn_s_sleep(4);
      }
      poll_flags<PBK>(a.fl + FL_G + (dT2 * 32 + (lane & 31)) * GSP, tag, lane >= 28);
      if (lane == 0) sy[11] = 2;
    } else word_wait<1>(sy + 11, 2);
    __builtin_amdgcn_s_setprio(3);
    gemm_main<14, L_ * 2, true, RW2, 3>(a.Gf + (size_t)(dT2 * 56 + w) * 4096, a.Gf + (size_t)(dT2 * 56 + (w ^ 2)) * 4096, wb2, vo, lring, tra, wr2, w, lane, acc,
                                        lds + w * (WL * 4096) + lane * 16, sy + 19);
    *(f32x4*)(lds + w * (WL * 4096) + 3 * 4096 + lane * 16) = acc[2][3];
    *(f32x4*)(lds + w * (WL * 4096) + 3 * 4096 + 1024 + lane * 16) = acc[3][3];
    grp_arrive(sy, w, 2, lane);
    EpiIn ei;
    epi_load(ei, a.x, a.b2, sTl, dT2, w, 0, lane);
    word_wait<0>(sy + 4 + w, 6);
    epi_load_s(ei, lds + X_SC, w, 0, lane);
    grp_wait<0>(sy, 2);
    epi_finish(ei, acc[0][3], acc[1][3], lds, a.out, w, 0, lane);
  } else {
    const int j4 = w - 4;
    volatile LDSP unsigned* xb = sy + 4;
    asm volatile("s_waitcnt vmcnt(2)" : "+v"(praw[0]), "+v"(praw[1]), "+v"(praw[2]), "+v"(praw[3]), "+v"(praw[4]), "+v"(praw[5]), "+v"(praw[6]), "+v"(praw[7]) :: "memory");
    const int ch = j4 >> 1, hw = j4 & 1;
    const int d = tb * 2 + ch;
    {
      float* par = (float*)(lds + X_SC + j4 * 2048);
      float* tabf = (float*)(lds + X_SC + 8192 + ch * (TABN * 8));
      const float dt = rbf(__expf(bf_lo(praw[6])));
      const float zr = dt * bf_lo(praw[0]) * LOG2E, zi = dt * bf_lo(praw[1]) * 0.15915494309189535f;
      const float br = dt * bf_lo(praw[2]), bi = dt * bf_lo(praw[3]), cr = bf_lo(praw[4]), ci = bf_lo(praw[5]);
      float wr = cr * br - ci * bi, wi = cr * bi + ci * br;
      const float zmax = wave_max_nn(fmaxf(zr, 0.f));
      const float wmax = wave_max_nn(fmaxf(fabsf(wr), fabsf(wi)));
      const unsigned we = (__float_as_uint(wmax) >> 23) & 0xff;
      const unsigned wec = we < 16 ? 16 : (we > 250 ? 250 : we);
      const float wsc = __uint_as_float((266u - wec) << 23);
      const float osc = __uint_as_float((wec - 12u) << 23) * 6.103515625e-05f;
      const float zs = zr - zmax;
      wr *= wsc; wi *= wsc;
      float qr, qi, er, ei;
      cexp2(zs * 128.f, zi * 128.f, qr, qi);
      cexp2(zs * 1024.f, zi * 1024.f, er, ei);
      *(f32x4*)(par + lane * 8) = f32x4{zs, zi, wr, wi};
      *(f32x4*)(par + lane * 8 + 4) = f32x4{qr, qi, er, ei};
      const float p2r = er * er - ei * ei, p2i = 2.f * er * ei;
      const float k2048 = wave_sum(wr * p2r - wi * p2i) * 16384.f;
      const int r = lane & 15, g = lane >> 4;
      const float va = 8.f * r;
      const int off = r < 8 ? r : r < 12 ? 256 + 2 * (r - 8) : 512 + 256 * ((r - 12) >> 1) + 4 * (r & 1);
      const float vb = (float)off;
      f32x4 acc[2][2];
#pragma unroll
      for (int t = 0; t < 4; ++t) acc[t >> 1][t & 1] = f32x4{0.f, 0.f, 0.f, 0.f};
#pragma unroll 1
      for (int ks = 0; ks < 4; ++ks) {
        u32x4 a0, a1, b0, b1;
#pragma unroll
        for (int j = 0; j < 4; ++j) {
          const int n = ks * 16 + g * 4 + j;
          const f32x4 pp = *(const f32x4*)(par + n * 8), pq = *(const f32x4*)(par + n * 8 + 4);
          float tr, ti, xr_, xi_;
          cexp2(__builtin_fmaf(pp[0], va, 14.f), pp[1] * va, tr, ti);
          cexp2(pp[0] * vb, pp[1] * vb, xr_, xi_);
          const float ur = pp[2] * xr_ - pp[3] * xi_, ui = pp[2] * xi_ + pp[3] * xr_;
          a0[j] = pk_f16(tr, ti);
          a1[j] = pk_f16(tr * pq[0] - ti * pq[1], tr * pq[1] + ti * pq[0]);
          b0[j] = pk_f16(ur, -ui);
          b1[j] = pk_f16(ur * pq[2] - ui * pq[3], -(ur * pq[3] + ui * pq[2]));
        }
        acc[0][0] = __builtin_amdgcn_mfma_f32_16x16x32_f16(__builtin_bit_cast(f16x8, a0), __builtin_bit_cast(f16x8, b0), acc[0][0], 0, 0, 0);
        acc[1][0] = __builtin_amdgcn_mfma_f32_16x16x32_f16(__builtin_bit_cast(f16x8, a1), __builtin_bit_cast(f16x8, b0), acc[1][0], 0, 0, 0);
        acc[0][1] = __builtin_amdgcn_mfma_f32_16x16x32_f16(__builtin_bit_cast(f16x8, a0), __builtin_bit_cast(f16x8, b1), acc[0][1], 0, 0, 0);
        acc[1][1] = __builtin_amdgcn_mfma_f32_16x16x32_f16(__builtin_bit_cast(f16x8, a1), __builtin_bit_cast(f16x8, b1), acc[1][1], 0, 0, 0);
      }
      float kl = k2048 * osc;
#pragma unroll
      for (int t = 0; t < 4; ++t) acc[t >> 1][t & 1] *= osc;
      if (zmax > 0.f) {
#pragma unroll
        for (int t = 0; t < 4; ++t)
#pragma unroll
          for (int e = 0; e < 4; ++e)
            acc[t >> 1][t & 1][e] *= __builtin_amdgcn_exp2f(zmax * (vb + (float)(8 * (16 * (t >> 1) + e) + 1024 * (t & 1)) + 32.f * g));
        kl *= __builtin_amdgcn_exp2f(zmax * 2048.f);
      }
      {
        const int stride = r < 8 ? 8 : r < 12 ? 4 : 2;
        const int base = r < 8 ? r : r < 12 ? 256 + (r - 8) : 384 + 64 * ((r - 12) >> 1) + (r & 1);
        float* p0 = tabf + (base + stride * 4 * g) * 2 + hw;
#pragma unroll
        for (int it = 0; it < 2; ++it)
#pragma unroll
          for (int e = 0; e < 4; ++e) p0[stride * (16 * it + e) * 2] = acc[it][0][e];
        if ((off & 255) == 0) {
          float* p1 = tabf + (512 + (off >> 3) + 4 * g) * 2 + hw;
#pragma unroll
          for (int it = 0; it < 2; ++it)
#pragma unroll
            for (int e = 0; e < 4; ++e) p1[(16 * it + e) * 2] = acc[it][1][e];
        }
        if (lane < 2) tabf[(640 + lane) * 2 + hw] = lane ? 0.f : kl;
      }
      grp_arrive(xb, j4, 1, lane);
      const int l2 = hw * 64 + lane;
      char* lk = lds + X_KT + ch * KT_B;
      int ix[17];
#pragma unroll
      for (int e = 0; e < 17; e += 2) {
        const int j0 = 2047 - 16 * l2 - e, j1 = j0 - 1;
        const unsigned bb = pk_bf16((float)j0, (float)j1);
        const int i0 = (int)(bb & 0xffff) - (0x4300 - 128), i1 = (int)(bb >> 16) - (0x4300 - 128);
        ix[e] = j0 < 128 ? (j0 < 0 ? 641 : j0) : i0;
        if (e + 1 < 17) ix[e + 1] = j1 < 128 ? j1 : i1;
      }
      grp_wait<1>(sy + 12, 1);
      if (l2 < 8) {
        const int pc = (l2 & 2) ? 258 + (l2 & 1) : (l2 & 1);
        *(u32x4*)(lk + (l2 >> 2) * R1OFF + pc * 16) = u32x4{0, 0, 0, 0};
      }
      if (j4 == 0) *(u32x4*)(lds + X_U + (lane & 1) * U_B + (lane >> 1) * 16) = u32x4{0, 0, 0, 0};
#pragma unroll
      for (int e = 0; e < 17; ++e) asm volatile("" : "+v"(ix[e]));
      grp_wait<0>(xb, 1);
      f32x2 tv[17];
#pragma unroll
      for (int e = 0; e < 17; ++e) tv[e] = *(const f32x2*)(tabf + ix[e] * 2);
      __builtin_amdgcn_sched_barrier(0);
      float val[17];
#pragma unroll
      for (int e = 0; e < 17; ++e) val[e] = tv[e][0] + tv[e][1];
#pragma unroll
      for (int hf = 0; hf < 2; ++hf) {
        u32x4 o0, o1;
#pragma unroll
        for (int e = 0; e < 4; ++e) { o0[e] = pk_bf16(val[8 * hf + 2 * e], val[8 * hf + 2 * e + 1]); o1[e] = pk_bf16(val[8 * hf + 2 * e + 1], val[8 * hf + 2 * e + 2]); }
        *(u32x4*)(lk + 32 + l2 * 32 + hf * 16) = o0;
        *(u32x4*)(lk + R1OFF + 32 + l2 * 32 + hf * 16) = o1;
      }
      if (l2 == 0) *(unsigned*)(lk + R1OFF + 28) = pk_bf16(0.f, val[0]);
    }
    {
      const float dsk = bf_lo(praw[7]);
      const char* lk = lds + X_KT + ch * KT_B;
      const char* lu = lds + X_U + ch * U_B;
      const int col = lane & 15, g = lane >> 4;
      const int p00 = 2047 - col + 8 * g;
      const char* gbase = lk + ((p00 & 1) ? R1OFF : 0) + (p00 >> 1) * 4 - 256 * hw;
      const char* bbase = lu + (UPAD - 16 + 16 * col + 8 * g) * 2 - 256 * hw;
      grp_bar<1>(xb, j4, 2, lane);
      bf16x8 gf[4][8], bf[2][8];
#pragma unroll
      for (int rr = 0; rr < 4; ++rr)
#pragma unroll
        for (int i = 0; i < 8; ++i) {
          const unsigned* gp = (const unsigned*)(gbase - 64 * (8 * i + rr));
          u32x4 gv = {gp[0], gp[1], gp[2], gp[3]};
          gf[rr][i] = __builtin_bit_cast(bf16x8, gv);
        }
#pragma unroll
      for (int rr = 0; rr < 4; ++rr)
        asm volatile("" : "+v"(gf[rr][0]), "+v"(gf[rr][1]), "+v"(gf[rr][2]), "+v"(gf[rr][3]), "+v"(gf[rr][4]), "+v"(gf[rr][5]), "+v"(gf[rr][6]), "+v"(gf[rr][7]));
      unsigned vo2[4];
      w_offsets<L_ * 2>(lane, vo2);
      word_wait(sy + 11, 1);
      if (has1) grp_wait<1>(sy + 20, 1);
      {
        u32x4 u0, u1;
        const int t0 = j4 * 64 + (lane >> 1);
        asm volatile("global_load_dwordx4 %0, %2, off sc1\n\tglobal_load_dwordx4 %1, %3, off sc1\n\ts_waitcnt vmcnt(0)" : "=&v"(u0), "=&v"(u1)
                     : "v"(a.X2 + ((size_t)t0 * 512 + tb * 2 + (lane & 1)) * 16), "v"(a.X2 + ((size_t)(t0 + 32) * 512 + tb * 2 + (lane & 1)) * 16) : "memory");
        char* lw = lds + X_U + (lane & 1) * U_B;
        *(u32x4*)(lw + UPAD * 2 + t0 * 16) = u0;
        *(u32x4*)(lw + UPAD * 2 + (t0 + 32) * 16) = u1;
      }
      grp_arrive(xb, j4, 3, lane);
      if (has1) w_issue<L_ * 2, 3>((const char*)a.w2 + (size_t)(j4 * 32) * (L_ * 2) + sTl * 128, vo2, lbase + j4 * (WL * 4096), 0, XS2);
      if (b == 0 && tid == 256) st4_dev(a.fl, tag);
      grp_wait<0>(xb, 3);
#define BLD(set, rr) do { _Pragma("unroll") for (int i = 0; i < 8; ++i) bf[set][i] = *(const bf16x8*)(bbase + 64 * (8 * i - (rr))); } while (0)
      BLD(0, 0);
      BLD(1, 1);
      f32x4 acc[8];
#pragma unroll
      for (int i = 0; i < 4; ++i) {
        const u32x2 uu = *(const u32x2*)(lu + (UPAD + 256 * (2 * i + hw) + 16 * col + 4 * g) * 2);
        const f32x4 uf = {bf_lo(uu[0]), bf_hi(uu[0]), bf_lo(uu[1]), bf_hi(uu[1])};
        if (hw) { acc[2 * i] = f32x4{0.f, 0.f, 0.f, 0.f}; acc[2 * i + 1] = uf * dsk; } else { acc[2 * i] = uf * dsk; acc[2 * i + 1] = f32x4{0.f, 0.f, 0.f, 0.f}; }
      }
      if (has1) {
        asm volatile("s_waitcnt vmcnt(0)" ::: "memory");
        if (lane == 0) sy[28 + j4] = 1;
        grp_wait<1>(sy + 24, 1);
      }
#pragma unroll
      for (int rr = 0; rr < 4; ++rr) {
#pragma unroll
        for (int bq = 0; bq < 8; ++bq)
#pragma unroll
          for (int i = 0; i + bq < 8; ++i) acc[i + bq] = __builtin_amdgcn_mfma_f32_16x16x32_bf16(gf[rr][bq], bf[rr & 1][i], acc[i + bq], 0, 0, 0);
        asm volatile("" : "+v"(acc[0]), "+v"(acc[1]), "+v"(acc[2]), "+v"(acc[3]), "+v"(acc[4]), "+v"(acc[5]), "+v"(acc[6]), "+v"(acc[7]) :: "memory");
        if (rr + 2 < 4) BLD(rr & 1, rr + 2);
        __builtin_amdgcn_sched_barrier(0);
      }
#undef BLD
      __builtin_amdgcn_s_setprio(3);
      char* xc = lds + X_SC + ch * 8192 + lane * 16;
      if (hw) {
#pragma unroll
        for (int i = 0; i < 4; ++i) { *(f32x4*)(xc + 4096 + i * 1024) = acc[2 * i]; acc[2 * i] = acc[2 * i + 1]; }
      } else {
#pragma unroll
        for (int i = 0; i < 4; ++i) *(f32x4*)(xc + i * 1024) = acc[2 * i + 1];
      }
      grp_arrive(xb, j4, 4, lane);
      word_wait<0>(xb + (j4 ^ 1), 4);
      u32x2 o[4];
#pragma unroll
      for (int i = 0; i < 4; ++i) {
        const f32x4 y = acc[2 * i] + *(const f32x4*)(xc + (hw ^ 1) * 4096 + i * 1024);
        o[i] = u32x2{pk_bf16(y[0], y[1]), pk_bf16(y[2], y[3])};
      }
#pragma unroll
      for (int ip = 0; ip < 2; ++ip) {
        u32x4 q4;
#pragma unroll
        for (int e = 0; e < 2; ++e) {
          auto sw = __builtin_amdgcn_permlane16_swap(o[2 * ip][e], o[2 * ip + 1][e], false, false);
          q4[e] = sw[0]; q4[2 + e] = sw[1];
        }
#pragma unroll
        for (int e = 0; e < 4; ++e) q4[e] = min(q4[e], 0xfffffffeu);
        const int t = 256 * (2 * (2 * ip + (g & 1)) + hw) + 16 * col + 8 * (g >> 1);
        st16_dev(a.sT + ((size_t)d * L_ + t) * 2, q4);
      }
      grp_arrive(xb, j4, 5, lane);
    }
    {
      __builtin_amdgcn_s_setprio(0);
      word_wait<2>(sy + 19, 1);
      grp_wait<1>(xb, 5);
      __builtin_amdgcn_s_setprio(3);
      EpiIn ei;
      epi_load(ei, a.x, a.b2, sTl, dT2, j4, 2, lane);
      u32x4 s0, s1;
      const int rw_ = j4 * 16 + (lane >> 3);
      const char* sp = a.sT + ((size_t)(dT2 * 64 + rw_) * L_ + sTl * 64) * 2 + (lane & 7) * 16;
#pragma unroll 1
      for (int att = 0; att < SPIN_CAP; ++att) {
        asm volatile("global_load_dwordx4 %0, %2, off sc1\n\tglobal_load_dwordx4 %1, %3, off sc1\n\ts_waitcnt vmcnt(0)" : "=&v"(s0), "=&v"(s1)
                     : "v"(sp), "v"(sp + (size_t)8 * L_ * 2) : "memory");
        const unsigned mx = max(max(max(s0[0], s0[1]), max(s0[2], s0[3])), max(max(s1[0], s1[1]), max(s1[2], s1[3])));
        if (!__any(mx == ~0u)) break;
        __builtin_amdgcn_s_sleep(8);
      }
      *(u32x4*)(lds + X_SC + rw_ * 144 + (lane & 7) * 16) = s0;
      *(u32x4*)(lds + X_SC + (rw_ + 8) * 144 + (lane & 7) * 16) = s1;
      grp_arrive(xb, j4, 6, lane);
      epi_load_s(ei, lds + X_SC, j4, 2, lane);
      asm volatile("s_waitcnt vmcnt(0) lgkmcnt(0)" ::: "memory");
      __builtin_amdgcn_s_setprio(0);
      grp_wait<1>(sy, 2);
      __builtin_amdgcn_s_setprio(3);
      const char* ow = lds + j4 * (WL * 4096) + 3 * 4096 + lane * 16;
      epi_finish(ei, *(const f32x4*)ow, *(const f32x4*)(ow + 1024), lds, a.out, j4, 2, lane);
    }
  }
}

__device__ __attribute__((noinline)) void code_end_marker() { asm volatile("s_nop 0"); }

extern "C" void kernel_launch(void* const* d_in, const int* in_sizes, int n_in, void* d_out, int out_size, void* d_ws,
                              size_t ws_size, hipStream_t stream) {
  char* ws = (char*)d_ws;
  Args a;
  a.x = (const u32x4*)d_in[0];
  a.Are = (cus)d_in[1]; a.Aim = (cus)d_in[2]; a.Bre = (cus)d_in[3]; a.Bim = (cus)d_in[4]; a.Cre = (cus)d_in[5]; a.Cim = (cus)d_in[6];
  a.logdt = (cus)d_in[7]; a.Dskip = (cus)d_in[8];
  a.w1 = (cus)d_in[10]; a.b1 = (cus)d_in[11]; a.w2 = (cus)d_in[12]; a.b2 = (cus)d_in[13];
  a.out = (unsigned short*)d_out;
  a.X2 = ws; a.Hf = ws + (2u << 20); a.Gf = ws + (4u << 20); a.sT = ws + (6u << 20); a.fl = (unsigned*)(ws + (8u << 20));
  (void)hipFuncSetAttribute((const void*)k_mega, hipFuncAttributeMaxDynamicSharedMemorySize, LDS_B);
  k_mega<<<256, 512, LDS_B, stream>>>(a);
}
```
